```python
import torch

def _softmax(x, axis=-1):
    return torch.softmax(x, dim=axis)
def _gelu(x):
    return torch.nn.functional.gelu(x, approximate="tanh")
def _silu(x):
    return torch.nn.functional.silu(x)
def _sigmoid(x):
    return torch.sigmoid(x)
def _layernorm(x, axis=-1, eps=1e-5):
    m = x.mean(dim=axis, keepdim=True)
    v = x.var(dim=axis, keepdim=True, unbiased=False)
    return (x - m) / torch.sqrt(v + eps)
def _scatter_add(tgt, idx, src, axis=0):
    return tgt.clone().index_add_(axis, idx, src)
def _segment_sum(src, seg, n):
    out = torch.zeros((n,) + src.shape[1:], dtype=src.dtype)
    return out.index_add_(0, seg, src)
def _pad(x, pw):
    flat = tuple(v for pair in reversed(pw) for v in pair)
    return torch.nn.functional.pad(x, flat)

kernel_name = 'standalone__paged_block_table_kv_pagedattention__decode'

def setup_inputs():

    _w = (lambda a: (lambda t: t.to(torch.bfloat16) if t.is_floating_point() else t)(torch.as_tensor(a)))
    B, Hq, Hkv, Sq, Skv, D = 2, 32, 4, 1, 32768, 128
    bs = 16
    nbps = max(1, Skv // bs)
    num_phys = B * nbps + 4
    bt = torch.randperm(num_phys)[: B * nbps].reshape(B, nbps).to(torch.int64)
    cl = (nbps * bs - (torch.arange(1, B + 1, dtype=torch.int64) * 2654435761) % max(1, nbps * bs // 2)).to(torch.int64)
    return {
        "q": _w((lambda *s: torch.randn(*s, dtype=torch.bfloat16) * 0.8)(B, Hq, Sq, D)),
        "k_cache": _w((lambda *s: torch.randn(*s, dtype=torch.bfloat16) * 0.8)(num_phys, bs, Hkv, D)),
        "v_cache": _w((lambda *s: torch.randn(*s, dtype=torch.bfloat16) * 0.05)(num_phys, bs, Hkv, D)),
        "block_table": _w(bt),
        "context_lens": _w(cl),
    }

def reference(q, k_cache, v_cache, block_table, context_lens):
    B, Hq, Sq, D = q.shape
    bs = k_cache.shape[1]
    Hkv = k_cache.shape[2]
    nbps = block_table.shape[1]
    Skv = nbps * bs
    bt = block_table.reshape(-1)
    k = (lambda x, *d: x.permute(*d))(k_cache[bt].reshape(B, Skv, Hkv, D), 0, 2, 1, 3)
    v = (lambda x, *d: x.permute(*d))(v_cache[bt].reshape(B, Skv, Hkv, D), 0, 2, 1, 3)
    g = Hq // Hkv
    qg = q.reshape(B, Hkv, g, Sq, D)
    scores = torch.einsum("bhgqd,bhkd->bhgqk", qg, k) * (D ** -0.5)
    pos = torch.arange(Skv)
    msk = pos[None, :] < context_lens[:, None]
    scores = torch.where(msk[:, None, None, None, :], scores, -1e9)
    p = _softmax(scores, axis=-1)
    out = torch.einsum("bhgqk,bhkd->bhgqd", p, v)
    return out.reshape(B, Hq, Sq, D)
```

```cpp
#include <hip/hip_runtime.h>
#include <hip/hip_bf16.h>
#include <stdint.h>

typedef __attribute__((ext_vector_type(4))) unsigned u32x4;
typedef __attribute__((ext_vector_type(4))) float f32x4;
typedef __attribute__((ext_vector_type(2))) float f32x2;
typedef __attribute__((ext_vector_type(8))) __bf16 bf16x8;

constexpr int NB = 2, HQ = 32, HKV = 4, HD = 128, PAGE = 16, GQ = 8;
constexpr int NBPS = 2048, SKV = NBPS * PAGE;
constexpr int NWAVE = 8, NTHR = NWAVE * 64;
constexpr int NSPLIT = 32;
constexpr int NSLOT = NSPLIT * NWAVE;
constexpr int NPW = 5;
constexpr int PAGE_BYTES = PAGE * HKV * HD * 2;
constexpr int TOK_BYTES = HKV * HD * 2;
#define KV_AUX 2

constexpr int L_SC = 80, L_SQ = 324, L_SW = 4 * L_SQ;
constexpr int L_ML = NWAVE * L_SW;

__device__ __forceinline__ f32x4 mfma16(u32x4 a, u32x4 b, f32x4 c) {
  return __builtin_amdgcn_mfma_f32_16x16x32_bf16(__builtin_bit_cast(bf16x8, a), __builtin_bit_cast(bf16x8, b), c, 0, 0, 0);
}
__device__ __forceinline__ unsigned cvtpk(float lo, float hi) {
  unsigned r;
  asm("v_cvt_pk_bf16_f32 %0, %1, %2" : "=v"(r) : "v"(lo), "v"(hi));
  return r;
}
__device__ __forceinline__ float xrow_max(float x) {
  auto s = __builtin_amdgcn_permlane16_swap(__float_as_uint(x), __float_as_uint(x), false, false);
  x = fmaxf(__uint_as_float(s[0]), __uint_as_float(s[1]));
  auto t = __builtin_amdgcn_permlane32_swap(__float_as_uint(x), __float_as_uint(x), false, false);
  return fmaxf(__uint_as_float(t[0]), __uint_as_float(t[1]));
}
__device__ __forceinline__ float xrow_sum(float x) {
  auto s = __builtin_amdgcn_permlane16_swap(__float_as_uint(x), __float_as_uint(x), false, false);
  x = __uint_as_float(s[0]) + __uint_as_float(s[1]);
  auto t = __builtin_amdgcn_permlane32_swap(__float_as_uint(x), __float_as_uint(x), false, false);
  return __uint_as_float(t[0]) + __uint_as_float(t[1]);
}

template <int N>
__device__ __forceinline__ void do_round(const unsigned char* __restrict__ kc, const unsigned char* __restrict__ vc,
                                         const int* phys, int rem0, int koff, int voff, int c, unsigned pmask, bool uni,
                                         const u32x4 (&qf)[4], float& m_run, float& l_run, f32x4 (&acc)[4]) {
  const float c2 = 0.088388347648318440f * 1.4426950408889634f;
  u32x4 kf[N][4], vf[N][4];
  __amdgpu_buffer_rsrc_t vr[N];
#pragma unroll
  for (int u = 0; u < N; ++u) {
    const int rem = rem0 - u * NSLOT * PAGE;
    const int nrec = (rem > 0 ? rem : 0) * TOK_BYTES;
    const size_t off = (size_t)(unsigned)phys[u] * PAGE_BYTES;
    __amdgpu_buffer_rsrc_t kr = __builtin_amdgcn_make_buffer_rsrc((void*)(kc + off), 0, nrec, 0x00020000);
    vr[u] = __builtin_amdgcn_make_buffer_rsrc((void*)(vc + off), 0, nrec, 0x00020000);
#pragma unroll
    for (int s = 0; s < 4; ++s) kf[u][s] = __builtin_amdgcn_raw_buffer_load_b128(kr, koff + 64 * s, 0, KV_AUX);
  }
#pragma unroll
  for (int u = 0; u < N; ++u)
#pragma unroll
    for (int j = 0; j < 4; ++j) vf[u][j] = __builtin_amdgcn_raw_buffer_load_b128(vr[u], voff + TOK_BYTES * j, 0, KV_AUX);
  __builtin_amdgcn_sched_barrier(0);

  float x[N][4];
  float mx = -1e30f;
#pragma unroll
  for (int u = 0; u < N; ++u) {
    const int rem = rem0 - u * NSLOT * PAGE;
    f32x4 s4 = f32x4{0.f, 0.f, 0.f, 0.f};
#pragma unroll
    for (int s = 0; s < 4; ++s) s4 = mfma16(kf[u][s], qf[s], s4);
#pragma unroll
    for (int r = 0; r < 4; ++r) {
      const float sc = uni ? 0.f : s4[r] * c2;
      x[u][r] = (4 * c + r < rem) ? sc : -INFINITY;
      mx = fmaxf(mx, x[u][r]);
    }
  }
  mx = xrow_max(mx);
  const float m_new = fmaxf(m_run, mx);
  const float alpha = __builtin_amdgcn_exp2f(m_run - m_new);
  m_run = m_new;
  l_run *= alpha;
#pragma unroll
  for (int k = 0; k < 4; ++k) acc[k] *= alpha;
  u32x4 pf[N];
#pragma unroll
  for (int u = 0; u < N; ++u)
#pragma unroll
    for (int r = 0; r < 4; ++r) {
      const float p = __builtin_amdgcn_exp2f(x[u][r] - m_new);
      l_run += p;
      pf[u][r] = cvtpk(p, p) & pmask;
    }
#pragma unroll
  for (int u = 0; u < N; ++u)
#pragma unroll
    for (int k = 0; k < 4; ++k) {
      const u32x4 a = u32x4{vf[u][0][k], vf[u][1][k], vf[u][2][k], vf[u][3][k]};
      acc[k] = mfma16(a, pf[u], acc[k]);
    }
}

__global__ void __launch_bounds__(NTHR, 2)
attn_split(const unsigned short* __restrict__ q, const unsigned char* __restrict__ kc, const unsigned char* __restrict__ vc,
           const int* __restrict__ bt, const int* __restrict__ lens, float* __restrict__ po, float* __restrict__ pml) {
  __shared__ __attribute__((aligned(16))) float lds[L_ML + NWAVE * 16];
  const int bid = blockIdx.x;
  const int kvh = bid & 3, b = (bid >> 2) & 1, g = bid >> 3;
  const int tid = threadIdx.x, lane = tid & 63;
  const int w = __builtin_amdgcn_readfirstlane(tid >> 6);
  const int c = lane >> 4, i = lane & 15;
  const int slot = w * NSPLIT + g;
  const int* btb = bt + b * NBPS + slot;

  int phys[NBPS / NSLOT];
#pragma unroll
  for (int u = 0; u < NBPS / NSLOT; ++u) phys[u] = btb[u * NSLOT];
  int len = lens[b];
  u32x4 qf[4];
  {
    const u32x4* qp = reinterpret_cast<const u32x4*>(q + (size_t)(b * HQ + kvh * GQ + (i & 7)) * HD + 8 * c);
#pragma unroll
    for (int s = 0; s < 4; ++s) qf[s] = qp[4 * s];
  }
  __builtin_amdgcn_sched_barrier(0);
  const bool uni = len <= 0;
  len = uni ? SKV : (len < SKV ? len : SKV);

  const int koff = i * TOK_BYTES + c * 16 + kvh * (HD * 2);
  const int voff = c * 4 * TOK_BYTES + i * 16 + kvh * (HD * 2);
  const unsigned pmask = (i & 8) ? 0xffff0000u : 0x0000ffffu;

  float m_run = -1e30f, l_run = 0.f;
  f32x4 acc[4];
#pragma unroll
  for (int k = 0; k < 4; ++k) acc[k] = f32x4{0.f, 0.f, 0.f, 0.f};

  const int rem0 = len - slot * PAGE;
  do_round<NPW>(kc, vc, phys, rem0, koff, voff, c, pmask, uni, qf, m_run, l_run, acc);
  if (rem0 > NPW * NSLOT * PAGE)
    do_round<NBPS / NSLOT - NPW>(kc, vc, phys + NPW, rem0 - NPW * NSLOT * PAGE, koff, voff, c, pmask, uni, qf, m_run, l_run, acc);
  l_run = xrow_sum(l_run);

#pragma unroll
  for (int k = 0; k < 4; ++k) *reinterpret_cast<f32x4*>(&lds[w * L_SW + k * L_SQ + c * L_SC + i * 4]) = acc[k];
  if (lane < 8) *reinterpret_cast<f32x2*>(&lds[L_ML + w * 16 + lane * 2]) = f32x2{m_run, l_run};
  __syncthreads();
  {
    const int h = w;
    float mw[NWAVE], lw[NWAVE];
    float M = -1e30f;
#pragma unroll
    for (int k = 0; k < NWAVE; ++k) {
      const f32x2 t = *reinterpret_cast<const f32x2*>(&lds[L_ML + k * 16 + h * 2]);
      mw[k] = t[0];
      lw[k] = t[1];
      M = fmaxf(M, mw[k]);
    }
    const int base = (lane & 3) * L_SQ + (lane >> 4) * L_SC + h * 4 + ((lane >> 2) & 3);
    float L = 0.f, o0 = 0.f, o1 = 0.f;
#pragma unroll
    for (int k = 0; k < NWAVE; ++k) {
      const float wt = __builtin_amdgcn_exp2f(mw[k] - M);
      L = fmaf(wt, lw[k], L);
      o0 = fmaf(wt, lds[k * L_SW + base], o0);
      o1 = fmaf(wt, lds[k * L_SW + base + 32], o1);
    }
    const int row = b * HQ + kvh * GQ + h;
    *reinterpret_cast<f32x2*>(&po[((size_t)row * NSPLIT + g) * HD + 2 * lane]) = f32x2{o0, o1};
    if (lane == 0) *reinterpret_cast<f32x2*>(&pml[((size_t)row * NSPLIT + g) * 2]) = f32x2{M, L};
  }
}

__global__ void __launch_bounds__(256)
attn_combine(const float* __restrict__ po, const float* __restrict__ pml, unsigned short* __restrict__ out) {
  __shared__ __attribute__((aligned(16))) float red[8 * HD];
  const int row = blockIdx.x, tid = threadIdx.x, sg = tid >> 5, o = tid & 31;
  f32x4 v[4];
#pragma unroll
  for (int k = 0; k < 4; ++k) v[k] = *reinterpret_cast<const f32x4*>(&po[((size_t)row * NSPLIT + sg * 4 + k) * HD + 4 * o]);
  const f32x2 ml = *reinterpret_cast<const f32x2*>(&pml[((size_t)row * NSPLIT + o) * 2]);
  float M = ml[0];
#pragma unroll
  for (int d = 16; d >= 1; d >>= 1) M = fmaxf(M, __shfl_xor(M, d));
  const float wt = __builtin_amdgcn_exp2f(ml[0] - M);
  float L = wt * ml[1];
#pragma unroll
  for (int d = 16; d >= 1; d >>= 1) L += __shfl_xor(L, d);
  f32x4 a = f32x4{0.f, 0.f, 0.f, 0.f};
#pragma unroll
  for (int k = 0; k < 4; ++k) a += v[k] * __shfl(wt, (tid & 32) + sg * 4 + k);
  *reinterpret_cast<f32x4*>(&red[sg * HD + 4 * o]) = a;
  __syncthreads();
  if (tid < 64) {
    float o0 = 0.f, o1 = 0.f;
#pragma unroll
    for (int k = 0; k < 8; ++k) {
      const f32x2 t = *reinterpret_cast<const f32x2*>(&red[k * HD + 2 * tid]);
      o0 += t[0];
      o1 += t[1];
    }
    const float inv = 1.f / L;
    reinterpret_cast<unsigned*>(out)[row * (HD / 2) + tid] = cvtpk(o0 * inv, o1 * inv);
  }
}

extern "C" void kernel_launch(void* const* d_in, const int* in_sizes, int n_in, void* d_out, int out_size, void* d_ws,
                              size_t ws_size, hipStream_t stream) {
  const unsigned short* q = (const unsigned short*)d_in[0];
  const unsigned char* kc = (const unsigned char*)d_in[1];
  const unsigned char* vc = (const unsigned char*)d_in[2];
  const int* bt = (const int*)d_in[3];
  const int* lens = (const int*)d_in[4];
  float* pml = (float*)d_ws;
  float* po = pml + NB * HQ * NSPLIT * 2;
  attn_split<<<NB * HKV * NSPLIT, NTHR, 0, stream>>>(q, kc, vc, bt, lens, po, pml);
  attn_combine<<<NB * HQ, 256, 0, stream>>>(po, pml, (unsigned short*)d_out);
}
```

```cpp
#include <hip/hip_runtime.h>
#include <stdint.h>

typedef __attribute__((ext_vector_type(2))) unsigned u32x2;
typedef __attribute__((ext_vector_type(4))) unsigned u32x4;
typedef __attribute__((ext_vector_type(16))) unsigned u32x16;
typedef __attribute__((ext_vector_type(2))) float f32x2;
typedef __attribute__((ext_vector_type(4))) float f32x4;
typedef __attribute__((ext_vector_type(2))) __bf16 bf16x2;
typedef __attribute__((ext_vector_type(8))) __bf16 bf16x8;
#define LAS __attribute__((address_space(3)))

constexpr int NB = 2, HQ = 32, HKV = 4, HD = 128, PAGE = 16, GQ = 8;
constexpr int NBPS = 2048, SKV = NBPS * PAGE;
constexpr int NWAVE = 8, NTHR = NWAVE * 64;
constexpr int NTEAM = 4, TEAM = 64, NCONS = 32;
constexpr int NSLOT = TEAM * 4;
constexpr int NPW = 5;
constexpr int LA = 4;
constexpr int PAGE_BYTES = PAGE * HKV * HD * 2, TOK_BYTES = HKV * HD * 2;
constexpr int AUX_NT = 2, RSRC3 = 0x00020000;

constexpr int WS_EPOCH = 0, WS_F = 1024, WS_O = WS_F + NTEAM * 16 * TEAM * 8;
constexpr int WS_END = WS_O + NTEAM * TEAM * NCONS * 128;

constexpr int L_SK = 1088, L_SW = 4 * L_SK;
constexpr int L_M = NWAVE * L_SW, L_L = L_M + 256, L_WG = L_L + 256, L_W2 = L_WG + 512, L_CNT = L_W2 + 256;
constexpr int L_LS = L_CNT + 256, L_G = L_LS + 512, L_END = L_G + (TEAM + 1) * 144;

__device__ __forceinline__ f32x4 mfma16(u32x4 a, u32x4 b, f32x4 c) {
  return __builtin_amdgcn_mfma_f32_16x16x32_bf16(__builtin_bit_cast(bf16x8, a), __builtin_bit_cast(bf16x8, b), c, 0, 0, 0);
}
__device__ __forceinline__ unsigned cvtpk(float lo, float hi) {
  return __builtin_bit_cast(unsigned, __builtin_convertvector(f32x2{lo, hi}, bf16x2));
}
constexpr int ROR8 = 0x128;
__device__ __forceinline__ float ror8(float x) {
  return __builtin_bit_cast(float, __builtin_amdgcn_mov_dpp(__builtin_bit_cast(int, x), ROR8, 0xf, 0xf, true));
}
__device__ __forceinline__ float max3(float a, float b, float c) { return fmaxf(fmaxf(a, b), c); }
__device__ __forceinline__ float xrow_max(float x) {
  auto s = __builtin_amdgcn_permlane16_swap(__float_as_uint(x), __float_as_uint(x), false, false);
  x = fmaxf(__uint_as_float(s[0]), __uint_as_float(s[1]));
  auto t = __builtin_amdgcn_permlane32_swap(__float_as_uint(x), __float_as_uint(x), false, false);
  return fmaxf(__uint_as_float(t[0]), __uint_as_float(t[1]));
}
__device__ __forceinline__ float xrow_sum(float x) {
  auto s = __builtin_amdgcn_permlane16_swap(__float_as_uint(x), __float_as_uint(x), false, false);
  x = __uint_as_float(s[0]) + __uint_as_float(s[1]);
  auto t = __builtin_amdgcn_permlane32_swap(__float_as_uint(x), __float_as_uint(x), false, false);
  return __uint_as_float(t[0]) + __uint_as_float(t[1]);
}
#define VSWAP(a, b) do { unsigned x_ = (a), y_ = (b); asm("v_swap_b32 %0, %1" : "+v"(x_), "+v"(y_)); (a) = x_; (b) = y_; } while (0)
__device__ __forceinline__ void transpose4(u32x4 (&v)[4]) {
  VSWAP(v[0][1], v[1][0]); VSWAP(v[0][2], v[2][0]); VSWAP(v[1][2], v[2][1]);
  VSWAP(v[0][3], v[3][0]); VSWAP(v[1][3], v[3][1]); VSWAP(v[2][3], v[3][2]);
  asm("s_nop 1" : "+v"(v[0]), "+v"(v[1]), "+v"(v[2]), "+v"(v[3]));
}
template <typename T> __device__ __forceinline__ void lds_st(LAS char* p, T v) { *reinterpret_cast<LAS T*>(p) = v; }

struct Epi {
  LAS char *ml_w, *acc_w, *l4, *g_r, *wg_w;
  unsigned a0, a1, a2, a3, a4, a5, a6, a7, t2;
  int out_off, lse_off;
  unsigned sel_lo, sel_hi;
};

template <int N, bool FIRST, typename F>
__device__ __forceinline__ void do_round(const unsigned char* kc, const unsigned char* vc, const int (&phys)[N], int rem0, int remk0,
                                         int lane, int koff, int voff, u32x4 (&qf)[2], float& m_run, f32x4 (&acc)[5], Epi& ep,
                                         F&& init_epi, bool last, float& lse, bool& mine) {
  const int c = lane >> 4;
  const float c2 = 0.088388347648318440f * 1.4426950408889634f;
  const u32x4 ones = u32x4{0x3f803f80u, 0x3f803f80u, 0x3f803f80u, 0x3f803f80u};
  u32x4 kf[N][4], vf[N][4], pf[N];
  float x[N][4];
  f32x4 d[2];
#pragma unroll
  for (int st = 0; st < 2 * N + LA; ++st) {
    const int g = st - LA;
    if (g == 0 && FIRST) {
      init_epi();
      asm volatile("" : "+v"(ep.ml_w), "+v"(ep.acc_w), "+v"(ep.l4), "+v"(ep.g_r), "+v"(ep.wg_w), "+v"(ep.a0), "+v"(ep.a1), "+v"(ep.a2),
                   "+v"(ep.a3), "+v"(ep.a4), "+v"(ep.a5), "+v"(ep.a6), "+v"(ep.a7), "+v"(ep.t2), "+v"(ep.out_off), "+v"(ep.lse_off),
                   "+v"(ep.sel_lo), "+v"(ep.sel_hi));
      const unsigned qm = remk0 != rem0 ? 0u : ~0u;
      qf[0] &= qm;
      qf[1] &= qm;
    }
    if (g >= 0 && g < N) {
#pragma unroll
      for (int hh = 0; hh < 2; ++hh) d[hh] = mfma16(kf[g][2 * hh], qf[0], f32x4{0.f, 0.f, 0.f, 0.f});
#pragma unroll
      for (int hh = 0; hh < 2; ++hh) d[hh] = mfma16(kf[g][2 * hh + 1], qf[1], d[hh]);
      __builtin_amdgcn_sched_barrier(0);
    }
    if (st < 2 * N) {
      const int u = st < N ? st : st - N;
      const int rem = (st < N ? remk0 : rem0) - u * NSLOT * PAGE;
      const int nrec = (rem > 0 ? rem : 0) * TOK_BYTES;
      const size_t off = (size_t)(unsigned)phys[u] * PAGE_BYTES;
      __amdgpu_buffer_rsrc_t r = __builtin_amdgcn_make_buffer_rsrc((void*)((st < N ? kc : vc) + off), 0, nrec, RSRC3);
#pragma unroll
      for (int s = 0; s < 4; ++s) {
        if (st < N) kf[u][s] = __builtin_amdgcn_raw_buffer_load_b128(r, koff + (s & 1) * 128 + (s >> 1) * TOK_BYTES, 0, AUX_NT);
        else vf[u][s] = __builtin_amdgcn_raw_buffer_load_b128(r, voff + TOK_BYTES * s, 0, AUX_NT);
      }
      __builtin_amdgcn_sched_barrier(0);
      if (FIRST && st == 0) __builtin_amdgcn_s_barrier();
    }
    if (g >= 0 && g < N) {
#pragma unroll
      for (int hh = 0; hh < 2; ++hh)
#pragma unroll
        for (int a = 0; a < 2; ++a) {
          const float t = d[hh][2 * a] + ror8(d[hh][2 * a + 1]);
          x[g][2 * a + hh] = __builtin_bit_cast(
              float, __builtin_amdgcn_update_dpp(__builtin_bit_cast(int, t), __builtin_bit_cast(int, t), ROR8, 0xf, 0xc, false));
        }
      __builtin_amdgcn_sched_barrier(0);
    }
    if (g == N - 1) {
      if (rem0 - (N - 1) * NSLOT * PAGE < PAGE) {
#pragma unroll
        for (int u = 0; u < N; ++u)
#pragma unroll
          for (int j = 0; j < 4; ++j) x[u][j] = (4 * c + j < rem0 - u * NSLOT * PAGE) ? x[u][j] : -INFINITY;
      }
      float mx = -INFINITY;
#pragma unroll
      for (int u = 0; u < N; ++u) mx = max3(max3(mx, x[u][0], x[u][1]), x[u][2], x[u][3]);
      const float m_new = fmaxf(m_run, xrow_max(mx) * c2);
      if (!FIRST) {
        const float alpha = __builtin_amdgcn_exp2f(m_run - m_new);
#pragma unroll
        for (int k = 0; k < 5; ++k) acc[k] *= alpha;
      }
      m_run = m_new;
#pragma unroll
      for (int u = 0; u < N; ++u) {
#pragma unroll
        for (int j = 0; j < 4; j += 2) {
          const unsigned pk = cvtpk(__builtin_amdgcn_exp2f(fmaf(x[u][j], c2, -m_new)), __builtin_amdgcn_exp2f(fmaf(x[u][j + 1], c2, -m_new)));
          pf[u][j] = __builtin_amdgcn_perm(0u, pk, ep.sel_lo);
          pf[u][j + 1] = __builtin_amdgcn_perm(0u, pk, ep.sel_hi);
        }
        acc[4] = mfma16(ones, pf[u], acc[4]);
      }
      __builtin_amdgcn_sched_barrier(0);
    }
    if (g == N) {
      lds_st<float>(ep.ml_w, m_run);
      lds_st<float>(ep.ml_w + (L_L - L_M), acc[4][0]);
      unsigned old;
      float mj, lj;
      asm volatile("ds_add_rtn_u32 %0, %3, %4 offset:%5\n\tds_read_b32 %1, %3 offset:%6\n\tds_read_b32 %2, %3 offset:%7\n\ts_waitcnt lgkmcnt(0)"
                   : "=&v"(old), "=&v"(mj), "=&v"(lj)
                   : "v"(ep.l4), "v"(last ? 1u : 0u), "i"(L_CNT), "i"(L_M), "i"(L_L)
                   : "memory");
      mine = last && __builtin_amdgcn_readfirstlane(old) == NWAVE - 1;
      const float M = xrow_max(mj);
      const float wt = __builtin_amdgcn_exp2f(mj - M);
      const float L = xrow_sum(wt * lj);
      lds_st<float>(ep.wg_w + (mine ? 0 : 256), L > 0.f ? wt * __builtin_amdgcn_rcpf(L) : 0.f);
      lse = M + __builtin_amdgcn_logf(L);
      asm volatile("" : "+v"(lse));
      __builtin_amdgcn_sched_barrier(0);
    }
    if (g >= N) {
      const int u = g - N;
      transpose4(vf[u]);
#pragma unroll
      for (int k = 0; k < 4; ++k) acc[k] = mfma16(vf[u][k], pf[u], acc[k]);
      __builtin_amdgcn_sched_barrier(0);
    }
  }
}

__global__ void __launch_bounds__(NTHR, 2)
attn_decode(const int* bt_, const int* lens_, unsigned char* ws_, const unsigned short* q_, const unsigned char* kc_,
            const unsigned char* vc_, unsigned short* out_, const void* pad_) {
  __shared__ __attribute__((aligned(16))) char lds_[L_END];
  LAS char* lds = (LAS char*)lds_;
  u32x16 ka;
  asm volatile("s_load_dwordx16 %0, %1, 0x0" : "=s"(ka) : "s"(__builtin_amdgcn_kernarg_segment_ptr()));
  unsigned cw = (threadIdx.x & 63) * 128;
  {
    unsigned long long pc;
    asm volatile("s_getpc_b64 %1\n.Lattn_pc:\n\tv_min_u32 %0, .Lattn_end-.Lattn_pc, %0\n\tglobal_load_dword %0, %0, %1" : "+v"(cw), "=&s"(pc));
  }
  const int bid = blockIdx.x;
  const int sw = (bid >> 3) & 1, team = (bid & 1) * 2 + sw, rank = ((bid >> 1) & 3) | ((bid >> 4) << 2);
  const int w = __builtin_amdgcn_readfirstlane(threadIdx.x >> 6);
  asm volatile("s_cmp_lg_u32 %0, 7\n\ts_cbranch_scc1 .Lattn_nt\n\ts_mov_b64 exec, 0\n\ts_branch .Lattn_isl\n.Lattn_back:\n\ts_mov_b64 exec, -1\n.Lattn_nt:"
               :: "s"(w) : "scc");
  const int b = w & 1, kvh = team ^ b;
  const int slot = (w >> 1) * TEAM + (rank ^ (b * (TEAM - 1)));
  const int bt_off = (b * NBPS + slot) * 4;
  asm volatile("s_waitcnt lgkmcnt(0)" : "+s"(ka) : "s"(bt_off), "s"(b * 4), "s"(WS_EPOCH + team * 256));
#define KPTR(n) (((unsigned long long)ka[2 * (n) + 1] << 32) | ka[2 * (n)])
  int phys[NPW], len;
  unsigned epoch;
  static_assert(NPW == 5 && NSLOT * 4 == 0x400, "block-table offsets below");
  asm volatile(
      "s_load_dword %0, %7, %10 offset:0x0\n\ts_load_dword %5, %8, %11 offset:0x0\n\ts_load_dword %1, %7, %10 offset:0x400\n\t"
      "s_load_dword %2, %7, %10 offset:0x800\n\ts_load_dword %3, %7, %10 offset:0xc00\n\ts_load_dword %4, %7, %10 offset:0x1000\n\t"
      "s_load_dword %6, %9, %12 offset:0x0"
      : "=&s"(phys[0]), "=&s"(phys[1]), "=&s"(phys[2]), "=&s"(phys[3]), "=&s"(phys[4]), "=&s"(len), "=&s"(epoch)
      : "s"(KPTR(0)), "s"(KPTR(1)), "s"(KPTR(2)), "s"(bt_off), "s"(b * 4), "s"(WS_EPOCH + team * 256));
  int tid = threadIdx.x;
  asm volatile("" : "+v"(tid));
  const int lane = tid & 63, c = lane >> 4, i = lane & 15;
  lds_st<unsigned>(lds + L_CNT + lane * 4, 0u);
  lds_st<unsigned>(lds + L_LS + 4 + lane * 8, 0u);
  const unsigned char* kc = (const unsigned char*)KPTR(4);
  const unsigned char* vc = (const unsigned char*)KPTR(5);
  u32x4 qf[2];
  {
    __amdgpu_buffer_rsrc_t qr = __builtin_amdgcn_make_buffer_rsrc((void*)KPTR(3), 0, NB * HQ * HD * 2, RSRC3);
#pragma unroll
    for (int s = 0; s < 2; ++s)
      qf[s] = __builtin_amdgcn_raw_buffer_load_b128(qr, (i & 7) * (HD * 2) + c * 32 + (i >> 3) * 16 + s * 128, (b * HQ + kvh * GQ) * HD * 2, 0);
  }
  asm volatile(".Lattn_isl:\n\ts_cbranch_execz .Lattn_back");
  const int koff = (i >> 1) * (2 * TOK_BYTES) + c * 32 + (i & 1) * 16 + kvh * (HD * 2);
  const int voff = c * 4 * TOK_BYTES + i * 16 + kvh * (HD * 2);
  __builtin_amdgcn_sched_barrier(0);
  asm volatile("s_waitcnt lgkmcnt(0)"
               : "+s"(phys[0]), "+s"(phys[1]), "+s"(phys[2]), "+s"(phys[3]), "+s"(phys[4]), "+s"(len), "+s"(epoch)
               : "v"(koff), "v"(voff));
  epoch += 0x4001u;
  const unsigned tag = (epoch * 0x9E3779B1u + 0x7F4A7C15u) | 1u;
  float m_run = -1e30f;
  f32x4 acc[5];
#pragma unroll
  for (int k = 0; k < 5; ++k) acc[k] = f32x4{0.f, 0.f, 0.f, 0.f};

  const bool uni = len <= 0;
  len = uni ? SKV : (len < SKV ? len : SKV);
  const int rem0 = len - slot * PAGE, remk0 = (uni ? 0 : len) - slot * PAGE;

  Epi ep;
  auto init_epi = [&]() {
    const int p8 = lane >> 3, e = lane & 7;
    ep.ml_w = lds + L_M + ((w >> 1) * 16 + b * 8 + e) * 4;
    ep.wg_w = lds + L_WG + i * 16 + c * 4;
    ep.acc_w = lds + w * L_SW + lane * 16;
    ep.l4 = lds + lane * 4;
    ep.g_r = lds + L_G + lane * 144 + w * 16;
    const int hd = (w >> 1) & 1, ch = w & 1, cons = (hd * 8 + p8) * 2 + ch;
    const int p0 = (w & 3) * 16 + p8, p1 = p0 + 8;
    const bool mg = w < 4;
    const unsigned lb = (unsigned)(size_t)lds;
    const int pa = mg ? rank : p0, ca = mg ? cons : rank;
    const unsigned o0 = WS_O + ((team * TEAM + pa) * NCONS + ((ca + pa) & (NCONS - 1))) * 128 + e * 16;
    const unsigned o1 = WS_O + ((team * TEAM + p1) * NCONS + ((rank + p1) & (NCONS - 1))) * 128 + e * 16;
    ep.a0 = mg ? lb + L_WG + (hd * 8 + p8) * 16 : WS_F + ((team * 16 + (rank >> 1)) * TEAM + (w & 3) * 16 + i) * 8;
    ep.a1 = (!mg && p0 == rank) ? WS_END : o0;
    ep.a2 = mg ? lb + hd * L_SW + (e >> 1) * L_SK + (ch * 32 + (e & 1) * 8 + p8) * 16 : (p1 == rank ? WS_END : o1);
    ep.a3 = lb + L_G + (mg ? (cons == rank ? rank : TEAM) : (p0 == rank ? TEAM : p0)) * 144 + e * 16;
    ep.a4 = lb + L_G + (p1 == rank ? TEAM : p1) * 144 + e * 16;
    ep.a5 = mg ? 0x4b002000u : (p0 == rank ? 0u : 0x3c000u);
    ep.a6 = mg ? ((rank | lane) == 0 ? WS_EPOCH + team * 256 : WS_END) : (p1 == rank ? 0u : 0x3c000u);
    ep.a7 = lb + L_LS + (mg ? lane : (w & 3) * 16 + i) * 8;
    ep.t2 = (epoch + (e << 15)) & 0x3c000u;
    const int ob = rank >> 4;
    ep.out_off = (e == 0) ? ((ob * HQ + (team ^ ob) * GQ + ((rank >> 1) & 7)) * HD + (rank & 1) * 64 + p8 * 8 + w) * 2 : NB * HQ * HD * 2;
    ep.lse_off = lane < 16 ? WS_F + ((team * 16 + lane) * TEAM + rank) * 8 : WS_END;
    ep.sel_lo = (lane & 8) ? 0x01000c0cu : 0x0c0c0100u;
    ep.sel_hi = (lane & 8) ? 0x03020c0cu : 0x0c0c0302u;
  };

  float lse;
  bool mine;
  do_round<NPW, true>(kc, vc, phys, rem0, remk0, lane, koff, voff, qf, m_run, acc, ep, init_epi, rem0 <= NPW * NSLOT * PAGE, lse, mine);
  if (__builtin_expect(rem0 > NPW * NSLOT * PAGE, 0)) {
    constexpr int N2 = NBPS / NSLOT - NPW;
    int phys2[N2];
#pragma unroll
    for (int u = 0; u < N2; ++u) phys2[u] = ((const __attribute__((address_space(4))) int*)KPTR(0))[b * NBPS + slot + (NPW + u) * NSLOT];
    do_round<N2, false>(kc, vc, phys2, rem0 - NPW * NSLOT * PAGE, remk0 - NPW * NSLOT * PAGE, lane, koff, voff, qf, m_run, acc, ep,
                        init_epi, true, lse, mine);
  }
  asm volatile("" ::"v"(cw));

#pragma unroll
  for (int k = 0; k < 4; ++k) lds_st<f32x4>(ep.acc_w + k * L_SK, acc[k]);

#define DPPF " row_mask:0xf bank_mask:0xf\n\t"
#define WAVE_RED(op)                                             \
  op "_dpp v13, v12, v12 quad_perm:[1,0,3,2]" DPPF "s_nop 1\n\t" \
  op "_dpp v13, v13, v13 quad_perm:[2,3,0,1]" DPPF "s_nop 1\n\t" \
  op "_dpp v13, v13, v13 row_half_mirror" DPPF "s_nop 1\n\t"     \
  op "_dpp v14, v13, v13 row_ror:8" DPPF                         \
  op "_dpp v13, v13, v13 row_ror:8" DPPF "s_nop 1\n\t"           \
  "v_permlane16_swap_b32 v13, v14\n\t"                           \
  op " v15, v13, v14\n\t" op " v13, v13, v14\n\ts_nop 1\n\t"     \
  "v_permlane32_swap_b32 v13, v15\n\t"                           \
  op " v13, v13, v15\n\t"
#define POLL(lo, hi) "v_mov_b32 v" #lo ", 0\n\tv_mov_b32 v" #hi ", 0\n\tbuffer_atomic_or_x2 v[" #lo ":" #hi "], %[a0], %[wr], 0 offen sc0 sc1\n\t"
#define SEEN(lo, hi) "v_cmp_ne_u32 vcc, %[tag], v" #hi "\n\tds_write_b64 %[a7], v[" #lo ":" #hi "]\n\t"
  unsigned spins = 1u << 20, spins2 = 1u << 20;
  __amdgpu_buffer_rsrc_t wr = __builtin_amdgcn_make_buffer_rsrc((void*)KPTR(2), 0, WS_END, RSRC3);
  __amdgpu_buffer_rsrc_t orr = __builtin_amdgcn_make_buffer_rsrc((void*)KPTR(6), 0, NB * HQ * HD * 2, RSRC3);
  asm volatile(
      "s_cmp_gt_u32 %[w], 3\n\t"
      "s_waitcnt lgkmcnt(0)\n\t"
      "s_barrier\n\t"
      "buffer_store_dwordx2 %[lg], %[lo], %[wr], 0 offen sc1\n\t"
      "s_cbranch_scc1 10f\n\t"
      "ds_read_b128 v[4:7], %[a0]\n\t"
      "ds_read_b128 v[8:11], %[a2]\n\t"
      "ds_read_b128 v[12:15], %[a2] offset:256\n\t"
      "ds_read_b128 v[16:19], %[a2] offset:%[o1]\n\t"
      "ds_read_b128 v[20:23], %[a2] offset:%[o1]+256\n\t"
      "ds_read_b128 v[24:27], %[a2] offset:2*%[o1]\n\t"
      "ds_read_b128 v[28:31], %[a2] offset:2*%[o1]+256\n\t"
      "ds_read_b128 v[32:35], %[a2] offset:3*%[o1]\n\t"
      "ds_read_b128 v[36:39], %[a2] offset:3*%[o1]+256\n\t"
      "s_waitcnt lgkmcnt(6)\n\t"
      "v_pk_mul_f32 v[8:9], v[8:9], v[4:5] op_sel_hi:[1,0]\n\t"
      "v_pk_mul_f32 v[10:11], v[10:11], v[4:5] op_sel_hi:[1,0]\n\t"
      "v_pk_mul_f32 v[12:13], v[12:13], v[4:5] op_sel_hi:[1,0]\n\t"
      "v_pk_mul_f32 v[14:15], v[14:15], v[4:5] op_sel_hi:[1,0]\n\t"
      "s_waitcnt lgkmcnt(4)\n\t"
      "v_pk_fma_f32 v[8:9], v[16:17], v[4:5], v[8:9] op_sel:[0,1,0]\n\t"
      "v_pk_fma_f32 v[10:11], v[18:19], v[4:5], v[10:11] op_sel:[0,1,0]\n\t"
      "v_pk_fma_f32 v[12:13], v[20:21], v[4:5], v[12:13] op_sel:[0,1,0]\n\t"
      "v_pk_fma_f32 v[14:15], v[22:23], v[4:5], v[14:15] op_sel:[0,1,0]\n\t"
      "s_waitcnt lgkmcnt(2)\n\t"
      "v_pk_fma_f32 v[8:9], v[24:25], v[6:7], v[8:9] op_sel_hi:[1,0,1]\n\t"
      "v_pk_fma_f32 v[10:11], v[26:27], v[6:7], v[10:11] op_sel_hi:[1,0,1]\n\t"
      "v_pk_fma_f32 v[12:13], v[28:29], v[6:7], v[12:13] op_sel_hi:[1,0,1]\n\t"
      "v_pk_fma_f32 v[14:15], v[30:31], v[6:7], v[14:15] op_sel_hi:[1,0,1]\n\t"
      "s_waitcnt lgkmcnt(0)\n\t"
      "v_pk_fma_f32 v[8:9], v[32:33], v[6:7], v[8:9] op_sel:[0,1,0]\n\t"
      "v_pk_fma_f32 v[10:11], v[34:35], v[6:7], v[10:11] op_sel:[0,1,0]\n\t"
      "v_pk_fma_f32 v[12:13], v[36:37], v[6:7], v[12:13] op_sel:[0,1,0]\n\t"
      "v_pk_fma_f32 v[14:15], v[38:39], v[6:7], v[14:15] op_sel:[0,1,0]\n\t"
      "v_max3_f32 v16, |v8|, |v9|, |v10|\n\t"
      "v_max3_f32 v17, |v11|, |v12|, |v13|\n\t"
      "v_max3_f32 v18, |v14|, |v15|, %[emin]\n\t"
      "v_max3_f32 v16, v16, v17, v18\n\t"
      "v_mov_b32 v20, %[a5]\n\t"
      "v_and_b32 v16, 0x7f800000, v16\n\t"
      "v_sub_u32 v18, 0x84800000, v16\n\t"
      "v_lshrrev_b32 v17, 9, v16\n\t"
      "v_lshrrev_b32 v16, 13, v16\n\t"
      "v_pk_fma_f32 v[8:9], v[8:9], v[18:19], v[20:21] op_sel_hi:[1,0,0]\n\t"
      "v_pk_fma_f32 v[10:11], v[10:11], v[18:19], v[20:21] op_sel_hi:[1,0,0]\n\t"
      "v_pk_fma_f32 v[12:13], v[12:13], v[18:19], v[20:21] op_sel_hi:[1,0,0]\n\t"
      "v_pk_fma_f32 v[14:15], v[14:15], v[18:19], v[20:21] op_sel_hi:[1,0,0]\n\t"
      "v_and_b32 v17, 0x3c000, v17\n\t"
      "v_lshl_or_b32 v4, v9, 18, %[t2]\n\t"
      "v_lshl_or_b32 v5, v11, 18, v17\n\t"
      "v_lshl_or_b32 v6, v13, 18, %[t2]\n\t"
      "v_lshl_or_b32 v7, v15, 18, v16\n\t"
      "v_bfi_b32 v4, %[m14], v8, v4\n\t"
      "v_bfi_b32 v5, %[m14], v10, v5\n\t"
      "v_bfi_b32 v6, %[m14], v12, v6\n\t"
      "v_bfi_b32 v7, %[m14], v14, v7\n\t"
      "buffer_store_dwordx4 v[4:7], %[a1], %[wr], 0 offen sc1\n\t"
      "s_cmp_gt_u32 %[rk], %[nc]\n\t"
      "s_cbranch_scc1 30f\n\t"
      "ds_write_b128 %[a3], v[4:7]\n\t"
      "s_cmp_lg_u32 %[w], 0\n\t"
      "s_cbranch_scc1 20f\n"
      "1:\n\t"
      "ds_read_b64 v[12:13], %[a7]\n\t"
      "s_sub_u32 %[sp], %[sp], 1\n\t"
      "s_waitcnt lgkmcnt(0)\n\t"
      "s_cbranch_scc1 2f\n\t"
      "v_cmp_ne_u32 vcc, %[tag], v13\n\t"
      "s_sleep 1\n\t"
      "s_cbranch_vccnz 1b\n"
      "2:\n\t"
      "v_mov_b32 v16, %[ep]\n\t"
      "buffer_store_dword v16, %[a6], %[wr], 0 offen sc1\n\t"
      WAVE_RED("v_max_f32")
      "v_sub_f32 v12, v12, v13\n\t"
      "v_exp_f32 v12, v12\n\t"
      "s_nop 1\n\t"
      WAVE_RED("v_add_f32")
      "v_rcp_f32 v13, v13\n\t"
      "v_mul_f32 v12, 0x3a000000, v12\n\t"
      "v_mul_f32 v12, v12, v13\n\t"
      "ds_write_b32 %[l4], v12 offset:%[w2]\n\t"
      "s_branch 20f\n"
      "10:\n\t"
      "s_cmp_gt_u32 %[rk], %[nc]\n\t"
      "s_cbranch_scc1 30f\n\t"
      "s_mov_b64 exec, 0xffff\n\t"
      POLL(12, 13) "s_sleep 4\n\t"
      POLL(14, 15) "s_sleep 4\n"
      "11:\n\t"
      POLL(16, 17) "s_waitcnt vmcnt(2)\n\t" SEEN(12, 13) "s_cbranch_vccz 12f\n\t"
      POLL(12, 13) "s_waitcnt vmcnt(2)\n\t" SEEN(14, 15) "s_cbranch_vccz 12f\n\t"
      POLL(14, 15) "s_sub_u32 %[sp], %[sp], 1\n\ts_waitcnt vmcnt(2)\n\ts_cbranch_scc1 12f\n\t" SEEN(16, 17) "s_cbranch_vccnz 11b\n"
      "12:\n\t"
      "s_mov_b64 exec, -1\n\t"
      "13:\n\t"
      "buffer_load_dwordx4 v[4:7], %[a1], %[wr], 0 offen sc1\n\t"
      "buffer_load_dwordx4 v[8:11], %[a2], %[wr], 0 offen sc1\n\t"
      "s_sub_u32 %[sq], %[sq], 1\n\t"
      "s_waitcnt vmcnt(1)\n\t"
      "v_bitop3_b32 v20, v4, v6, %[t2] bitop3:0x7e\n\t"
      "ds_write_b128 %[a3], v[4:7]\n\t"
      "v_and_b32 v20, v20, %[a5]\n\t"
      "s_waitcnt vmcnt(0)\n\t"
      "ds_write_b128 %[a4], v[8:11]\n\t"
      "v_bitop3_b32 v21, v8, v10, %[t2] bitop3:0x7e\n\t"
      "v_bitop3_b32 v20, v21, v20, %[a6] bitop3:0xec\n\t"
      "s_cbranch_scc1 20f\n\t"
      "v_cmp_ne_u32 vcc, 0, v20\n\t"
      "s_cbranch_vccnz 13b\n\t"
      "20:\n\t"
      "s_waitcnt lgkmcnt(0)\n\t"
      "s_barrier\n\t"
      "ds_read_b128 v[4:7], %[grd]\n\t"
      "ds_read_b32 v12, %[l4] offset:%[w2]\n\t"
      "v_mov_b32 v16, 0x4b000000\n\t"
      "v_mov_b32 v14, 0xcb002000\n\t"
      "s_waitcnt lgkmcnt(1)\n\t"
      "v_bfe_u32 v13, v5, 14, 4\n\t"
      "v_bfe_u32 v15, v7, 14, 4\n\t"
      "v_and_or_b32 v8, v4, %[m14], v16\n\t"
      "v_and_or_b32 v9, v5, %[m14], v16\n\t"
      "v_lshl_or_b32 v13, v15, 4, v13\n\t"
      "v_and_or_b32 v10, v6, %[m14], v16\n\t"
      "v_and_or_b32 v11, v7, %[m14], v16\n\t"
      "v_lshlrev_b32 v13, 23, v13\n\t"
      "v_alignbit_b32 v4, %[mag], v4, 18\n\t"
      "v_alignbit_b32 v5, %[mag], v5, 18\n\t"
      "v_alignbit_b32 v6, %[mag], v6, 18\n\t"
      "v_alignbit_b32 v7, %[mag], v7, 18\n\t"
      "s_waitcnt lgkmcnt(0)\n\t"
      "v_mul_f32 v12, v12, v13\n\t"
      "v_pk_add_f32 v[8:9], v[8:9], v[14:15] op_sel_hi:[1,0]\n\t"
      "v_pk_add_f32 v[10:11], v[10:11], v[14:15] op_sel_hi:[1,0]\n\t"
      "v_pk_add_f32 v[4:5], v[4:5], v[14:15] op_sel_hi:[1,0]\n\t"
      "v_pk_add_f32 v[6:7], v[6:7], v[14:15] op_sel_hi:[1,0]\n\t"
      "v_pk_mul_f32 v[8:9], v[8:9], v[12:13] op_sel_hi:[1,0]\n\t"
      "v_pk_mul_f32 v[10:11], v[10:11], v[12:13] op_sel_hi:[1,0]\n\t"
      "v_pk_mul_f32 v[4:5], v[4:5], v[12:13] op_sel_hi:[1,0]\n\t"
      "v_pk_mul_f32 v[6:7], v[6:7], v[12:13] op_sel_hi:[1,0]\n\t"
      "v_permlane32_swap_b32 v8, v10\n\t"
      "v_permlane32_swap_b32 v9, v11\n\t"
      "v_permlane32_swap_b32 v4, v6\n\t"
      "v_permlane32_swap_b32 v5, v7\n\t"
      "v_pk_add_f32 v[8:9], v[8:9], v[10:11]\n\t"
      "v_pk_add_f32 v[4:5], v[4:5], v[6:7]\n\t"
      "s_nop 0\n\t"
      "v_permlane16_swap_b32 v8, v9\n\t"
      "v_permlane16_swap_b32 v4, v5\n\t"
      "v_add_f32 v8, v8, v9\n\t"
      "v_add_f32 v4, v4, v5\n\t"
      "s_nop 0\n\t"
      "v_add_f32_dpp v8, v8, v8 row_ror:8 row_mask:0xf bank_mask:0x3\n\t"
      "v_add_f32_dpp v8, v4, v4 row_ror:8 row_mask:0xf bank_mask:0xc\n\t"
      "s_nop 1\n\t"
      "v_add_f32_dpp v8, v8, v8 row_half_mirror" DPPF
      "s_nop 1\n\t"
      "v_add_f32_dpp v8, v8, v8 quad_perm:[2,3,0,1]" DPPF
      "s_nop 1\n\t"
      "v_add_f32_dpp v8, v8, v8 quad_perm:[1,0,3,2]" DPPF
      "v_cvt_pk_bf16_f32 v8, v8, v8\n\t"
      "buffer_store_short v8, %[oo], %[orr], 0 offen\n\t"
      "30:\n"
      ".Lattn_end:"
      : [sp] "+s"(spins), [sq] "+s"(spins2)
      : [wr] "s"(wr), [orr] "s"(orr), [lg] "v"(u32x2{__float_as_uint(lse), tag}), [lo] "v"(mine ? ep.lse_off : WS_END), [w] "s"(w),
        [rk] "s"(rank), [nc] "i"(NCONS - 1), [a0] "v"(ep.a0), [a1] "v"(ep.a1), [a2] "v"(ep.a2), [a3] "v"(ep.a3), [a4] "v"(ep.a4),
        [a5] "v"(ep.a5), [a6] "v"(ep.a6), [a7] "v"(ep.a7), [l4] "v"(ep.l4), [grd] "v"(ep.g_r), [oo] "v"(ep.out_off), [w2] "i"(L_W2),
        [o1] "i"(2 * L_SW), [tag] "s"(tag), [ep] "s"(epoch), [t2] "v"(ep.t2), [m14] "s"(0x3fffu), [mag] "s"(0x4b000000u >> 14),
        [emin] "s"(0x0a000000u)
      : "memory", "vcc", "scc", "v4", "v5", "v6", "v7", "v8", "v9", "v10", "v11", "v12", "v13", "v14", "v15", "v16", "v17", "v18", "v19",
        "v20", "v21", "v22", "v23", "v24", "v25", "v26", "v27", "v28", "v29", "v30", "v31", "v32", "v33", "v34", "v35", "v36", "v37",
        "v38", "v39");
}

extern "C" void kernel_launch(void* const* d_in, const int* in_sizes, int n_in, void* d_out, int out_size, void* d_ws,
                              size_t ws_size, hipStream_t stream) {
  attn_decode<<<NTEAM * TEAM, NTHR, 0, stream>>>((const int*)d_in[3], (const int*)d_in[4], (unsigned char*)d_ws,
                                                 (const unsigned short*)d_in[0], (const unsigned char*)d_in[1],
                                                 (const unsigned char*)d_in[2], (unsigned short*)d_out, d_ws);
}
```

```cpp
#include <hip/hip_runtime.h>
#include <stdint.h>

typedef __attribute__((ext_vector_type(2))) unsigned u32x2;
typedef __attribute__((ext_vector_type(4))) unsigned u32x4;
typedef __attribute__((ext_vector_type(16))) unsigned u32x16;
typedef __attribute__((ext_vector_type(2))) float f32x2;
typedef __attribute__((ext_vector_type(4))) float f32x4;
typedef __attribute__((ext_vector_type(2))) __bf16 bf16x2;
typedef __attribute__((ext_vector_type(8))) __bf16 bf16x8;
#define LAS __attribute__((address_space(3)))

constexpr int NB = 2, HQ = 32, HKV = 4, HD = 128, PAGE = 16, GQ = 8;
constexpr int NBPS = 2048, SKV = NBPS * PAGE;
constexpr int NWAVE = 8, NTHR = NWAVE * 64;
constexpr int NTEAM = 4, TEAM = 64, NCONS = 32;
constexpr int NSLOT = TEAM * 4;
constexpr int NPW = 5;
constexpr int LA = 4;
constexpr int PAGE_BYTES = PAGE * HKV * HD * 2, TOK_BYTES = HKV * HD * 2;
constexpr int AUX_NT = 2, RSRC3 = 0x00020000;

constexpr int WS_EPOCH = 0, WS_F = 1024, WS_O = WS_F + NTEAM * 16 * TEAM * 8;
constexpr int WS_END = WS_O + NTEAM * TEAM * NCONS * 128;

constexpr int L_SK = 1088, L_SW = 4 * L_SK;
constexpr int L_M = NWAVE * L_SW, L_L = L_M + 256, L_WG = L_L + 256, L_W2 = L_WG + 512, L_CNT = L_W2 + 256;
constexpr int L_LS = L_CNT + 256, L_G = L_LS + 512, L_END = L_G + (TEAM + 1) * 144;

__device__ __forceinline__ f32x4 mfma16(u32x4 a, u32x4 b, f32x4 c) {
  return __builtin_amdgcn_mfma_f32_16x16x32_bf16(__builtin_bit_cast(bf16x8, a), __builtin_bit_cast(bf16x8, b), c, 0, 0, 0);
}
__device__ __forceinline__ unsigned cvtpk(float lo, float hi) {
  return __builtin_bit_cast(unsigned, __builtin_convertvector(f32x2{lo, hi}, bf16x2));
}
constexpr int ROR8 = 0x128;
__device__ __forceinline__ float ror8(float x) {
  return __builtin_bit_cast(float, __builtin_amdgcn_mov_dpp(__builtin_bit_cast(int, x), ROR8, 0xf, 0xf, true));
}
__device__ __forceinline__ float max3(float a, float b, float c) { return fmaxf(fmaxf(a, b), c); }
__device__ __forceinline__ float xrow_max(float x) {
  auto s = __builtin_amdgcn_permlane16_swap(__float_as_uint(x), __float_as_uint(x), false, false);
  x = fmaxf(__uint_as_float(s[0]), __uint_as_float(s[1]));
  auto t = __builtin_amdgcn_permlane32_swap(__float_as_uint(x), __float_as_uint(x), false, false);
  return fmaxf(__uint_as_float(t[0]), __uint_as_float(t[1]));
}
__device__ __forceinline__ float xrow_sum(float x) {
  auto s = __builtin_amdgcn_permlane16_swap(__float_as_uint(x), __float_as_uint(x), false, false);
  x = __uint_as_float(s[0]) + __uint_as_float(s[1]);
  auto t = __builtin_amdgcn_permlane32_swap(__float_as_uint(x), __float_as_uint(x), false, false);
  return __uint_as_float(t[0]) + __uint_as_float(t[1]);
}
#define VSWAP(a, b) do { unsigned x_ = (a), y_ = (b); asm("v_swap_b32 %0, %1" : "+v"(x_), "+v"(y_)); (a) = x_; (b) = y_; } while (0)
__device__ __forceinline__ void transpose4(u32x4 (&v)[4]) {
  VSWAP(v[0][1], v[1][0]); VSWAP(v[0][2], v[2][0]); VSWAP(v[1][2], v[2][1]);
  VSWAP(v[0][3], v[3][0]); VSWAP(v[1][3], v[3][1]); VSWAP(v[2][3], v[3][2]);
  asm("s_nop 1" : "+v"(v[0]), "+v"(v[1]), "+v"(v[2]), "+v"(v[3]));
}
template <typename T> __device__ __forceinline__ void lds_st(LAS char* p, T v) { *reinterpret_cast<LAS T*>(p) = v; }

struct Epi {
  LAS char *ml_w, *acc_w, *l4, *g_r, *wg_w;
  unsigned a0, a1, a2, a3, a4, a5, a6, a7, t2;
  int out_off, lse_off;
  unsigned sel_lo, sel_hi;
};

template <int N, bool FIRST, typename F>
__device__ __forceinline__ void do_round(const unsigned char* kc, const unsigned char* vc, const int (&phys)[N], int rem0, int remk0,
                                         int lane, int koff, int voff, u32x4 (&qf)[2], float& m_run, f32x4 (&acc)[5], Epi& ep,
                                         F&& init_epi, bool last, float& lse, bool& mine) {
  const int c = lane >> 4;
  const float c2 = 0.088388347648318440f * 1.4426950408889634f;
  const u32x4 ones = u32x4{0x3f803f80u, 0x3f803f80u, 0x3f803f80u, 0x3f803f80u};
  u32x4 kf[N][4], vf[N][4], pf[N];
  float x[N][4];
  f32x4 d[2];
#pragma unroll
  for (int st = 0; st < 2 * N + LA; ++st) {
    const int g = st - LA;
    if (g == 0 && FIRST) {
      init_epi();
      asm volatile("" : "+v"(ep.ml_w), "+v"(ep.acc_w), "+v"(ep.l4), "+v"(ep.g_r), "+v"(ep.wg_w), "+v"(ep.a0), "+v"(ep.a1), "+v"(ep.a2),
                   "+v"(ep.a3), "+v"(ep.a4), "+v"(ep.a5), "+v"(ep.a6), "+v"(ep.a7), "+v"(ep.t2), "+v"(ep.out_off), "+v"(ep.lse_off),
                   "+v"(ep.sel_lo), "+v"(ep.sel_hi));
      const unsigned qm = remk0 != rem0 ? 0u : ~0u;
      qf[0] &= qm;
      qf[1] &= qm;
    }
    if (g >= 0 && g < N) {
#pragma unroll
      for (int hh = 0; hh < 2; ++hh) d[hh] = mfma16(kf[g][2 * hh], qf[0], f32x4{0.f, 0.f, 0.f, 0.f});
#pragma unroll
      for (int hh = 0; hh < 2; ++hh) d[hh] = mfma16(kf[g][2 * hh + 1], qf[1], d[hh]);
      __builtin_amdgcn_sched_barrier(0);
    }
    if (st < 2 * N) {
      const int u = st < N ? st : st - N;
      const int rem = (st < N ? remk0 : rem0) - u * NSLOT * PAGE;
      const int nrec = (rem > 0 ? rem : 0) * TOK_BYTES;
      const size_t off = (size_t)(unsigned)phys[u] * PAGE_BYTES;
      __amdgpu_buffer_rsrc_t r = __builtin_amdgcn_make_buffer_rsrc((void*)((st < N ? kc : vc) + off), 0, nrec, RSRC3);
#pragma unroll
      for (int s = 0; s < 4; ++s) {
        if (st < N) kf[u][s] = __builtin_amdgcn_raw_buffer_load_b128(r, koff + (s & 1) * 128 + (s >> 1) * TOK_BYTES, 0, AUX_NT);
        else vf[u][s] = __builtin_amdgcn_raw_buffer_load_b128(r, voff + TOK_BYTES * s, 0, AUX_NT);
      }
      __builtin_amdgcn_sched_barrier(0);
      if (FIRST && st == 0) __builtin_amdgcn_s_barrier();
    }
    if (g >= 0 && g < N) {
#pragma unroll
      for (int hh = 0; hh < 2; ++hh)
#pragma unroll
        for (int a = 0; a < 2; ++a) {
          const float t = d[hh][2 * a] + ror8(d[hh][2 * a + 1]);
          x[g][2 * a + hh] = __builtin_bit_cast(
              float, __builtin_amdgcn_update_dpp(__builtin_bit_cast(int, t), __builtin_bit_cast(int, t), ROR8, 0xf, 0xc, false));
        }
      __builtin_amdgcn_sched_barrier(0);
    }
    if (g == N - 1) {
      if (rem0 - (N - 1) * NSLOT * PAGE < PAGE) {
#pragma unroll
        for (int u = 0; u < N; ++u)
#pragma unroll
          for (int j = 0; j < 4; ++j) x[u][j] = (4 * c + j < rem0 - u * NSLOT * PAGE) ? x[u][j] : -INFINITY;
      }
      float mx = -INFINITY;
#pragma unroll
      for (int u = 0; u < N; ++u) mx = max3(max3(mx, x[u][0], x[u][1]), x[u][2], x[u][3]);
      const float m_new = fmaxf(m_run, xrow_max(mx) * c2);
      if (!FIRST) {
        const float alpha = __builtin_amdgcn_exp2f(m_run - m_new);
#pragma unroll
        for (int k = 0; k < 5; ++k) acc[k] *= alpha;
      }
      m_run = m_new;
#pragma unroll
      for (int u = 0; u < N; ++u) {
#pragma unroll
        for (int j = 0; j < 4; j += 2) {
          const unsigned pk = cvtpk(__builtin_amdgcn_exp2f(fmaf(x[u][j], c2, -m_new)), __builtin_amdgcn_exp2f(fmaf(x[u][j + 1], c2, -m_new)));
          pf[u][j] = __builtin_amdgcn_perm(0u, pk, ep.sel_lo);
          pf[u][j + 1] = __builtin_amdgcn_perm(0u, pk, ep.sel_hi);
        }
        acc[4] = mfma16(ones, pf[u], acc[4]);
      }
      __builtin_amdgcn_sched_barrier(0);
    }
    if (g == N) {
      lds_st<float>(ep.ml_w, m_run);
      lds_st<float>(ep.ml_w + (L_L - L_M), acc[4][0]);
      unsigned old;
      float mj, lj;
      asm volatile("ds_add_rtn_u32 %0, %3, %4 offset:%5\n\tds_read_b32 %1, %3 offset:%6\n\tds_read_b32 %2, %3 offset:%7\n\ts_waitcnt lgkmcnt(0)"
                   : "=&v"(old), "=&v"(mj), "=&v"(lj)
                   : "v"(ep.l4), "v"(last ? 1u : 0u), "i"(L_CNT), "i"(L_M), "i"(L_L)
                   : "memory");
      mine = last && __builtin_amdgcn_readfirstlane(old) == NWAVE - 1;
      const float M = xrow_max(mj);
      const float wt = __builtin_amdgcn_exp2f(mj - M);
      const float L = xrow_sum(wt * lj);
      lds_st<float>(ep.wg_w + (mine ? 0 : 256), L > 0.f ? wt * __builtin_amdgcn_rcpf(L) : 0.f);
      lse = M + __builtin_amdgcn_logf(L);
      asm volatile("" : "+v"(lse));
      __builtin_amdgcn_sched_barrier(0);
    }
    if (g >= N) {
      const int u = g - N;
      transpose4(vf[u]);
#pragma unroll
      for (int k = 0; k < 4; ++k) acc[k] = mfma16(vf[u][k], pf[u], acc[k]);
      __builtin_amdgcn_sched_barrier(0);
    }
  }
}

__global__ void __launch_bounds__(NTHR, 2)
attn_decode(const int* bt_, const int* lens_, unsigned char* ws_, const unsigned short* q_, const unsigned char* kc_,
            const unsigned char* vc_, unsigned short* out_, const void* pad_) {
  __shared__ __attribute__((aligned(16))) char lds_[L_END];
  LAS char* lds = (LAS char*)lds_;
  u32x16 ka;
  asm volatile("s_load_dwordx16 %0, %1, 0x0" : "=s"(ka) : "s"(__builtin_amdgcn_kernarg_segment_ptr()));
  unsigned cw = (threadIdx.x & 63) * 128;
  {
    unsigned long long pc;
    asm volatile("s_getpc_b64 %1\n.Lattn_pc:\n\tv_min_u32 %0, .Lattn_end-.Lattn_pc, %0\n\tglobal_load_dword %0, %0, %1" : "+v"(cw), "=&s"(pc));
  }
  const int bid = blockIdx.x;
  const int sw = (bid >> 3) & 1, team = (bid & 1) * 2 + sw, rank = ((bid >> 1) & 3) | ((bid >> 4) << 2);
  const int w = __builtin_amdgcn_readfirstlane(threadIdx.x >> 6);
  asm volatile("s_cmp_lg_u32 %0, 7\n\ts_cbranch_scc1 .Lattn_nt\n\ts_mov_b64 exec, 0\n\ts_branch .Lattn_isl\n.Lattn_back:\n\ts_mov_b64 exec, -1\n.Lattn_nt:"
               :: "s"(w) : "scc");
  const int b = w & 1, kvh = team ^ b;
  const int slot = (w >> 1) * TEAM + (rank ^ (b * (TEAM - 1)));
  const int bt_off = (b * NBPS + slot) * 4;
  asm volatile("s_waitcnt lgkmcnt(0)" : "+s"(ka) : "s"(bt_off), "s"(b * 4), "s"(WS_EPOCH + team * 256));
#define KPTR(n) (((unsigned long long)ka[2 * (n) + 1] << 32) | ka[2 * (n)])
  int phys[NPW], len;
  unsigned epoch;
  static_assert(NPW == 5 && NSLOT * 4 == 0x400, "block-table offsets below");
  asm volatile(
      "s_load_dword %0, %7, %10 offset:0x0\n\ts_load_dword %5, %8, %11 offset:0x0\n\ts_load_dword %1, %7, %10 offset:0x400\n\t"
      "s_load_dword %2, %7, %10 offset:0x800\n\ts_load_dword %3, %7, %10 offset:0xc00\n\ts_load_dword %4, %7, %10 offset:0x1000\n\t"
      "s_load_dword %6, %9, %12 offset:0x0"
      : "=&s"(phys[0]), "=&s"(phys[1]), "=&s"(phys[2]), "=&s"(phys[3]), "=&s"(phys[4]), "=&s"(len), "=&s"(epoch)
      : "s"(KPTR(0)), "s"(KPTR(1)), "s"(KPTR(2)), "s"(bt_off), "s"(b * 4), "s"(WS_EPOCH + team * 256));
  int tid = threadIdx.x;
  asm volatile("" : "+v"(tid));
  const int lane = tid & 63, c = lane >> 4, i = lane & 15;
  lds_st<unsigned>(lds + L_CNT + lane * 4, 0u);
  lds_st<unsigned>(lds + L_LS + 4 + lane * 8, 0u);
  const unsigned char* kc = (const unsigned char*)KPTR(4);
  const unsigned char* vc = (const unsigned char*)KPTR(5);
  u32x4 qf[2];
  {
    __amdgpu_buffer_rsrc_t qr = __builtin_amdgcn_make_buffer_rsrc((void*)KPTR(3), 0, NB * HQ * HD * 2, RSRC3);
#pragma unroll
    for (int s = 0; s < 2; ++s)
      qf[s] = __builtin_amdgcn_raw_buffer_load_b128(qr, (i & 7) * (HD * 2) + c * 32 + (i >> 3) * 16 + s * 128, (b * HQ + kvh * GQ) * HD * 2, 0);
  }
  asm volatile(".Lattn_isl:\n\ts_cbranch_execz .Lattn_back");
  const int koff = (i >> 1) * (2 * TOK_BYTES) + c * 32 + (i & 1) * 16 + kvh * (HD * 2);
  const int voff = c * 4 * TOK_BYTES + i * 16 + kvh * (HD * 2);
  __builtin_amdgcn_sched_barrier(0);
  asm volatile("s_waitcnt lgkmcnt(0)"
               : "+s"(phys[0]), "+s"(phys[1]), "+s"(phys[2]), "+s"(phys[3]), "+s"(phys[4]), "+s"(len), "+s"(epoch)
               : "v"(koff), "v"(voff));
  epoch += 0x4001u;
  const unsigned tag = (epoch * 0x9E3779B1u + 0x7F4A7C15u) | 1u;
  float m_run = -1e30f;
  f32x4 acc[5];
#pragma unroll
  for (int k = 0; k < 5; ++k) acc[k] = f32x4{0.f, 0.f, 0.f, 0.f};

  const bool uni = len <= 0;
  len = uni ? SKV : (len < SKV ? len : SKV);
  const int rem0 = len - slot * PAGE, remk0 = (uni ? 0 : len) - slot * PAGE;

  Epi ep;
  auto init_epi = [&]() {
    const int p8 = lane >> 3, e = lane & 7;
    ep.ml_w = lds + L_M + ((w >> 1) * 16 + b * 8 + e) * 4;
    ep.wg_w = lds + L_WG + i * 16 + c * 4;
    ep.acc_w = lds + w * L_SW + lane * 16;
    ep.l4 = lds + lane * 4;
    ep.g_r = lds + L_G + lane * 144 + w * 16;
    const int hd = (w >> 1) & 1, ch = w & 1, cons = (hd * 8 + p8) * 2 + ch;
    const int p0 = (w & 3) * 16 + p8, p1 = p0 + 8;
    const bool mg = w < 4;
    const unsigned lb = (unsigned)(size_t)lds;
    const int pa = mg ? rank : p0, ca = mg ? cons : rank;
    const unsigned o0 = WS_O + ((team * TEAM + pa) * NCONS + ((ca + pa) & (NCONS - 1))) * 128 + e * 16;
    const unsigned o1 = WS_O + ((team * TEAM + p1) * NCONS + ((rank + p1) & (NCONS - 1))) * 128 + e * 16;
    ep.a0 = mg ? lb + L_WG + (hd * 8 + p8) * 16 : WS_F + ((team * 16 + (rank >> 1)) * TEAM + (w & 3) * 16 + i) * 8;
    ep.a1 = (!mg && p0 == rank) ? WS_END : o0;
    ep.a2 = mg ? lb + hd * L_SW + (e >> 1) * L_SK + (ch * 32 + (e & 1) * 8 + p8) * 16 : (p1 == rank ? WS_END : o1);
    ep.a3 = lb + L_G + (mg ? (cons == rank ? rank : TEAM) : (p0 == rank ? TEAM : p0)) * 144 + e * 16;
    ep.a4 = lb + L_G + (p1 == rank ? TEAM : p1) * 144 + e * 16;
    ep.a5 = mg ? 0x4b002000u : (p0 == rank ? 0u : 0x3c000u);
    ep.a6 = mg ? ((rank | lane) == 0 ? WS_EPOCH + team * 256 : WS_END) : (p1 == rank ? 0u : 0x3c000u);
    ep.a7 = lb + L_LS + (mg ? lane : (w & 3) * 16 + i) * 8;
    ep.t2 = (epoch + (e << 15)) & 0x3c000u;
    const int ob = rank >> 4;
    ep.out_off = (e == 0) ? ((ob * HQ + (team ^ ob) * GQ + ((rank >> 1) & 7)) * HD + (rank & 1) * 64 + p8 * 8 + w) * 2 : NB * HQ * HD * 2;
    ep.lse_off = lane < 16 ? WS_F + ((team * 16 + lane) * TEAM + rank) * 8 : WS_END;
    ep.sel_lo = (lane & 8) ? 0x01000c0cu : 0x0c0c0100u;
    ep.sel_hi = (lane & 8) ? 0x03020c0cu : 0x0c0c0302u;
  };

  float lse;
  bool mine;
  do_round<NPW, true>(kc, vc, phys, rem0, remk0, lane, koff, voff, qf, m_run, acc, ep, init_epi, rem0 <= NPW * NSLOT * PAGE, lse, mine);
  if (__builtin_expect(rem0 > NPW * NSLOT * PAGE, 0)) {
    constexpr int N2 = NBPS / NSLOT - NPW;
    int phys2[N2];
#pragma unroll
    for (int u = 0; u < N2; ++u) phys2[u] = ((const __attribute__((address_space(4))) int*)KPTR(0))[b * NBPS + slot + (NPW + u) * NSLOT];
    do_round<N2, false>(kc, vc, phys2, rem0 - NPW * NSLOT * PAGE, remk0 - NPW * NSLOT * PAGE, lane, koff, voff, qf, m_run, acc, ep,
                        init_epi, true, lse, mine);
  }
  asm volatile("" ::"v"(cw));

#pragma unroll
  for (int k = 0; k < 4; ++k) lds_st<f32x4>(ep.acc_w + k * L_SK, acc[k]);

#define DPPF " row_mask:0xf bank_mask:0xf\n\t"
#define WAVE_RED(op)                                             \
  op "_dpp v13, v12, v12 quad_perm:[1,0,3,2]" DPPF "s_nop 1\n\t" \
  op "_dpp v13, v13, v13 quad_perm:[2,3,0,1]" DPPF "s_nop 1\n\t" \
  op "_dpp v13, v13, v13 row_half_mirror" DPPF "s_nop 1\n\t"     \
  op "_dpp v14, v13, v13 row_ror:8" DPPF                         \
  op "_dpp v13, v13, v13 row_ror:8" DPPF "s_nop 1\n\t"           \
  "v_permlane16_swap_b32 v13, v14\n\t"                           \
  op " v15, v13, v14\n\t" op " v13, v13, v14\n\ts_nop 1\n\t"     \
  "v_permlane32_swap_b32 v13, v15\n\t"                           \
  op " v13, v13, v15\n\t"
#define POLL(lo, hi) "v_mov_b32 v" #lo ", 0\n\tv_mov_b32 v" #hi ", 0\n\tbuffer_atomic_or_x2 v[" #lo ":" #hi "], %[a0], %[wr], 0 offen sc0 sc1\n\t"
#define SEEN(lo, hi) "v_cmp_ne_u32 vcc, %[tag], v" #hi "\n\tds_write_b64 %[a7], v[" #lo ":" #hi "]\n\t"
  unsigned spins = 1u << 20, spins2 = 1u << 20;
  __amdgpu_buffer_rsrc_t wr = __builtin_amdgcn_make_buffer_rsrc((void*)KPTR(2), 0, WS_END, RSRC3);
  __amdgpu_buffer_rsrc_t orr = __builtin_amdgcn_make_buffer_rsrc((void*)KPTR(6), 0, NB * HQ * HD * 2, RSRC3);
  asm volatile(
      "s_cmp_gt_u32 %[w], 3\n\t"
      "s_waitcnt lgkmcnt(0)\n\t"
      "s_barrier\n\t"
      "buffer_atomic_swap_x2 %[lg], %[lo], %[wr], 0 offen sc1\n\t"
      "s_cbranch_scc1 10f\n\t"
      "ds_read_b128 v[4:7], %[a0]\n\t"
      "ds_read_b128 v[8:11], %[a2]\n\t"
      "ds_read_b128 v[12:15], %[a2] offset:256\n\t"
      "ds_read_b128 v[16:19], %[a2] offset:%[o1]\n\t"
      "ds_read_b128 v[20:23], %[a2] offset:%[o1]+256\n\t"
      "ds_read_b128 v[24:27], %[a2] offset:2*%[o1]\n\t"
      "ds_read_b128 v[28:31], %[a2] offset:2*%[o1]+256\n\t"
      "ds_read_b128 v[32:35], %[a2] offset:3*%[o1]\n\t"
      "ds_read_b128 v[36:39], %[a2] offset:3*%[o1]+256\n\t"
      "s_waitcnt lgkmcnt(6)\n\t"
      "v_pk_mul_f32 v[8:9], v[8:9], v[4:5] op_sel_hi:[1,0]\n\t"
      "v_pk_mul_f32 v[10:11], v[10:11], v[4:5] op_sel_hi:[1,0]\n\t"
      "v_pk_mul_f32 v[12:13], v[12:13], v[4:5] op_sel_hi:[1,0]\n\t"
      "v_pk_mul_f32 v[14:15], v[14:15], v[4:5] op_sel_hi:[1,0]\n\t"
      "s_waitcnt lgkmcnt(4)\n\t"
      "v_pk_fma_f32 v[8:9], v[16:17], v[4:5], v[8:9] op_sel:[0,1,0]\n\t"
      "v_pk_fma_f32 v[10:11], v[18:19], v[4:5], v[10:11] op_sel:[0,1,0]\n\t"
      "v_pk_fma_f32 v[12:13], v[20:21], v[4:5], v[12:13] op_sel:[0,1,0]\n\t"
      "v_pk_fma_f32 v[14:15], v[22:23], v[4:5], v[14:15] op_sel:[0,1,0]\n\t"
      "s_waitcnt lgkmcnt(2)\n\t"
      "v_pk_fma_f32 v[8:9], v[24:25], v[6:7], v[8:9] op_sel_hi:[1,0,1]\n\t"
      "v_pk_fma_f32 v[10:11], v[26:27], v[6:7], v[10:11] op_sel_hi:[1,0,1]\n\t"
      "v_pk_fma_f32 v[12:13], v[28:29], v[6:7], v[12:13] op_sel_hi:[1,0,1]\n\t"
      "v_pk_fma_f32 v[14:15], v[30:31], v[6:7], v[14:15] op_sel_hi:[1,0,1]\n\t"
      "s_waitcnt lgkmcnt(0)\n\t"
      "v_pk_fma_f32 v[8:9], v[32:33], v[6:7], v[8:9] op_sel:[0,1,0]\n\t"
      "v_pk_fma_f32 v[10:11], v[34:35], v[6:7], v[10:11] op_sel:[0,1,0]\n\t"
      "v_pk_fma_f32 v[12:13], v[36:37], v[6:7], v[12:13] op_sel:[0,1,0]\n\t"
      "v_pk_fma_f32 v[14:15], v[38:39], v[6:7], v[14:15] op_sel:[0,1,0]\n\t"
      "v_max3_f32 v16, |v8|, |v9|, |v10|\n\t"
      "v_max3_f32 v17, |v11|, |v12|, |v13|\n\t"
      "v_max3_f32 v18, |v14|, |v15|, %[emin]\n\t"
      "v_max3_f32 v16, v16, v17, v18\n\t"
      "v_mov_b32 v20, %[a5]\n\t"
      "v_and_b32 v16, 0x7f800000, v16\n\t"
      "v_sub_u32 v18, 0x84800000, v16\n\t"
      "v_lshrrev_b32 v17, 9, v16\n\t"
      "v_lshrrev_b32 v16, 13, v16\n\t"
      "v_pk_fma_f32 v[8:9], v[8:9], v[18:19], v[20:21] op_sel_hi:[1,0,0]\n\t"
      "v_pk_fma_f32 v[10:11], v[10:11], v[18:19], v[20:21] op_sel_hi:[1,0,0]\n\t"
      "v_pk_fma_f32 v[12:13], v[12:13], v[18:19], v[20:21] op_sel_hi:[1,0,0]\n\t"
      "v_pk_fma_f32 v[14:15], v[14:15], v[18:19], v[20:21] op_sel_hi:[1,0,0]\n\t"
      "v_and_b32 v17, 0x3c000, v17\n\t"
      "v_lshl_or_b32 v4, v9, 18, %[t2]\n\t"
      "v_lshl_or_b32 v5, v11, 18, v17\n\t"
      "v_lshl_or_b32 v6, v13, 18, %[t2]\n\t"
      "v_lshl_or_b32 v7, v15, 18, v16\n\t"
      "v_bfi_b32 v4, %[m14], v8, v4\n\t"
      "v_bfi_b32 v5, %[m14], v10, v5\n\t"
      "v_bfi_b32 v6, %[m14], v12, v6\n\t"
      "v_bfi_b32 v7, %[m14], v14, v7\n\t"
      "buffer_store_dwordx4 v[4:7], %[a1], %[wr], 0 offen sc0 sc1\n\t"
      "s_cmp_gt_u32 %[rk], %[nc]\n\t"
      "s_cbranch_scc1 30f\n\t"
      "ds_write_b128 %[a3], v[4:7]\n\t"
      "s_cmp_lg_u32 %[w], 0\n\t"
      "s_cbranch_scc1 20f\n"
      "1:\n\t"
      "ds_read_b64 v[12:13], %[a7]\n\t"
      "s_sub_u32 %[sp], %[sp], 1\n\t"
      "s_waitcnt lgkmcnt(0)\n\t"
      "s_cbranch_scc1 2f\n\t"
      "v_cmp_ne_u32 vcc, %[tag], v13\n\t"
      "s_sleep 1\n\t"
      "s_cbranch_vccnz 1b\n"
      "2:\n\t"
      "v_mov_b32 v16, %[ep]\n\t"
      "buffer_store_dword v16, %[a6], %[wr], 0 offen sc1\n\t"
      WAVE_RED("v_max_f32")
      "v_sub_f32 v12, v12, v13\n\t"
      "v_exp_f32 v12, v12\n\t"
      "s_nop 1\n\t"
      WAVE_RED("v_add_f32")
      "v_rcp_f32 v13, v13\n\t"
      "v_mul_f32 v12, 0x3a000000, v12\n\t"
      "v_mul_f32 v12, v12, v13\n\t"
      "ds_write_b32 %[l4], v12 offset:%[w2]\n\t"
      "s_branch 20f\n"
      "10:\n\t"
      "s_cmp_gt_u32 %[rk], %[nc]\n\t"
      "s_cbranch_scc1 30f\n\t"
      "s_mov_b64 exec, 0xffff\n\t"
      POLL(12, 13) "s_sleep 2\n\t"
      POLL(14, 15) "s_sleep 2\n"
      "11:\n\t"
      POLL(16, 17) "s_waitcnt vmcnt(2)\n\t" SEEN(12, 13) "s_cbranch_vccz 12f\n\t"
      POLL(12, 13) "s_waitcnt vmcnt(2)\n\t" SEEN(14, 15) "s_cbranch_vccz 12f\n\t"
      POLL(14, 15) "s_sub_u32 %[sp], %[sp], 1\n\ts_waitcnt vmcnt(2)\n\ts_cbranch_scc1 12f\n\t" SEEN(16, 17) "s_cbranch_vccnz 11b\n"
      "12:\n\t"
      "s_mov_b64 exec, -1\n\t"
      "13:\n\t"
      "buffer_load_dwordx4 v[4:7], %[a1], %[wr], 0 offen sc1\n\t"
      "buffer_load_dwordx4 v[8:11], %[a2], %[wr], 0 offen sc1\n\t"
      "s_sub_u32 %[sq], %[sq], 1\n\t"
      "s_waitcnt vmcnt(1)\n\t"
      "v_bitop3_b32 v20, v4, v6, %[t2] bitop3:0x7e\n\t"
      "ds_write_b128 %[a3], v[4:7]\n\t"
      "v_and_b32 v20, v20, %[a5]\n\t"
      "s_waitcnt vmcnt(0)\n\t"
      "ds_write_b128 %[a4], v[8:11]\n\t"
      "v_bitop3_b32 v21, v8, v10, %[t2] bitop3:0x7e\n\t"
      "v_bitop3_b32 v20, v21, v20, %[a6] bitop3:0xec\n\t"
      "s_cbranch_scc1 20f\n\t"
      "v_cmp_ne_u32 vcc, 0, v20\n\t"
      "s_cbranch_vccnz 13b\n\t"
      "20:\n\t"
      "s_waitcnt lgkmcnt(0)\n\t"
      "s_barrier\n\t"
      "ds_read_b128 v[4:7], %[grd]\n\t"
      "ds_read_b32 v12, %[l4] offset:%[w2]\n\t"
      "v_mov_b32 v16, 0x4b000000\n\t"
      "v_mov_b32 v14, 0xcb002000\n\t"
      "s_waitcnt lgkmcnt(1)\n\t"
      "v_bfe_u32 v13, v5, 14, 4\n\t"
      "v_bfe_u32 v15, v7, 14, 4\n\t"
      "v_and_or_b32 v8, v4, %[m14], v16\n\t"
      "v_and_or_b32 v9, v5, %[m14], v16\n\t"
      "v_lshl_or_b32 v13, v15, 4, v13\n\t"
      "v_and_or_b32 v10, v6, %[m14], v16\n\t"
      "v_and_or_b32 v11, v7, %[m14], v16\n\t"
      "v_lshlrev_b32 v13, 23, v13\n\t"
      "v_alignbit_b32 v4, %[mag], v4, 18\n\t"
      "v_alignbit_b32 v5, %[mag], v5, 18\n\t"
      "v_alignbit_b32 v6, %[mag], v6, 18\n\t"
      "v_alignbit_b32 v7, %[mag], v7, 18\n\t"
      "s_waitcnt lgkmcnt(0)\n\t"
      "v_mul_f32 v12, v12, v13\n\t"
      "v_pk_add_f32 v[8:9], v[8:9], v[14:15] op_sel_hi:[1,0]\n\t"
      "v_pk_add_f32 v[10:11], v[10:11], v[14:15] op_sel_hi:[1,0]\n\t"
      "v_pk_add_f32 v[4:5], v[4:5], v[14:15] op_sel_hi:[1,0]\n\t"
      "v_pk_add_f32 v[6:7], v[6:7], v[14:15] op_sel_hi:[1,0]\n\t"
      "v_pk_mul_f32 v[8:9], v[8:9], v[12:13] op_sel_hi:[1,0]\n\t"
      "v_pk_mul_f32 v[10:11], v[10:11], v[12:13] op_sel_hi:[1,0]\n\t"
      "v_pk_mul_f32 v[4:5], v[4:5], v[12:13] op_sel_hi:[1,0]\n\t"
      "v_pk_mul_f32 v[6:7], v[6:7], v[12:13] op_sel_hi:[1,0]\n\t"
      "v_permlane32_swap_b32 v8, v10\n\t"
      "v_permlane32_swap_b32 v9, v11\n\t"
      "v_permlane32_swap_b32 v4, v6\n\t"
      "v_permlane32_swap_b32 v5, v7\n\t"
      "v_pk_add_f32 v[8:9], v[8:9], v[10:11]\n\t"
      "v_pk_add_f32 v[4:5], v[4:5], v[6:7]\n\t"
      "s_nop 0\n\t"
      "v_permlane16_swap_b32 v8, v9\n\t"
      "v_permlane16_swap_b32 v4, v5\n\t"
      "v_add_f32 v8, v8, v9\n\t"
      "v_add_f32 v4, v4, v5\n\t"
      "s_nop 0\n\t"
      "v_add_f32_dpp v8, v8, v8 row_ror:8 row_mask:0xf bank_mask:0x3\n\t"
      "v_add_f32_dpp v8, v4, v4 row_ror:8 row_mask:0xf bank_mask:0xc\n\t"
      "s_nop 1\n\t"
      "v_add_f32_dpp v8, v8, v8 row_half_mirror" DPPF
      "s_nop 1\n\t"
      "v_add_f32_dpp v8, v8, v8 quad_perm:[2,3,0,1]" DPPF
      "s_nop 1\n\t"
      "v_add_f32_dpp v8, v8, v8 quad_perm:[1,0,3,2]" DPPF
      "v_cvt_pk_bf16_f32 v8, v8, v8\n\t"
      "buffer_store_short v8, %[oo], %[orr], 0 offen\n\t"
      "30:\n"
      ".Lattn_end:"
      : [sp] "+s"(spins), [sq] "+s"(spins2)
      : [wr] "s"(wr), [orr] "s"(orr), [lg] "v"(u32x2{__float_as_uint(lse), tag}), [lo] "v"(mine ? ep.lse_off : WS_END), [w] "s"(w),
        [rk] "s"(rank), [nc] "i"(NCONS - 1), [a0] "v"(ep.a0), [a1] "v"(ep.a1), [a2] "v"(ep.a2), [a3] "v"(ep.a3), [a4] "v"(ep.a4),
        [a5] "v"(ep.a5), [a6] "v"(ep.a6), [a7] "v"(ep.a7), [l4] "v"(ep.l4), [grd] "v"(ep.g_r), [oo] "v"(ep.out_off), [w2] "i"(L_W2),
        [o1] "i"(2 * L_SW), [tag] "s"(tag), [ep] "s"(epoch), [t2] "v"(ep.t2), [m14] "s"(0x3fffu), [mag] "s"(0x4b000000u >> 14),
        [emin] "s"(0x0a000000u)
      : "memory", "vcc", "scc", "v4", "v5", "v6", "v7", "v8", "v9", "v10", "v11", "v12", "v13", "v14", "v15", "v16", "v17", "v18", "v19",
        "v20", "v21", "v22", "v23", "v24", "v25", "v26", "v27", "v28", "v29", "v30", "v31", "v32", "v33", "v34", "v35", "v36", "v37",
        "v38", "v39");
}

extern "C" void kernel_launch(void* const* d_in, const int* in_sizes, int n_in, void* d_out, int out_size, void* d_ws,
                              size_t ws_size, hipStream_t stream) {
  attn_decode<<<NTEAM * TEAM, NTHR, 0, stream>>>((const int*)d_in[3], (const int*)d_in[4], (unsigned char*)d_ws,
                                                 (const unsigned short*)d_in[0], (const unsigned char*)d_in[1],
                                                 (const unsigned char*)d_in[2], (unsigned short*)d_out, d_ws);
}
```

```cpp
#include <hip/hip_runtime.h>
#include <stdint.h>

typedef __attribute__((ext_vector_type(2))) unsigned u32x2;
typedef __attribute__((ext_vector_type(4))) unsigned u32x4;
typedef __attribute__((ext_vector_type(16))) unsigned u32x16;
typedef __attribute__((ext_vector_type(2))) float f32x2;
typedef __attribute__((ext_vector_type(4))) float f32x4;
typedef __attribute__((ext_vector_type(2))) __bf16 bf16x2;
typedef __attribute__((ext_vector_type(8))) __bf16 bf16x8;
#define LAS __attribute__((address_space(3)))

constexpr int NB = 2, HQ = 32, HKV = 4, HD = 128, PAGE = 16, GQ = 8;
constexpr int NBPS = 2048, SKV = NBPS * PAGE;
constexpr int NWAVE = 8, NTHR = NWAVE * 64;
constexpr int NTEAM = 4, TEAM = 64, NCONS = 32;
constexpr int NSLOT = TEAM * 4;
constexpr int NPW = 5;
constexpr int LA = 3;
constexpr int TICKS = 118;
constexpr int TICK0 = 170 - 2 * TICKS;
constexpr int PAGE_BYTES = PAGE * HKV * HD * 2, TOK_BYTES = HKV * HD * 2;
constexpr int AUX_NT = 2, AUX_SC1 = 16, RSRC3 = 0x00020000;

constexpr int WS_EPOCH = 0, WS_T = 128, WS_F = 1024, WS_O = WS_F + NTEAM * 16 * TEAM * 8;
constexpr int WS_END = WS_O + NTEAM * TEAM * NCONS * 128;

constexpr int L_SK = 1088, L_SW = 4 * L_SK;
constexpr int L_M = NWAVE * L_SW, L_L = L_M + 256, L_WG = L_L + 256, L_W2 = L_WG + 512, L_CNT = L_W2 + 256;
constexpr int L_LS = L_CNT + 256, L_G = L_LS + 512, L_END = L_G + (TEAM + 1) * 144;

__device__ __forceinline__ f32x4 mfma16(u32x4 a, u32x4 b, f32x4 c) {
  return __builtin_amdgcn_mfma_f32_16x16x32_bf16(__builtin_bit_cast(bf16x8, a), __builtin_bit_cast(bf16x8, b), c, 0, 0, 0);
}
__device__ __forceinline__ unsigned cvtpk(float lo, float hi) {
  return __builtin_bit_cast(unsigned, __builtin_convertvector(f32x2{lo, hi}, bf16x2));
}
constexpr int ROR8 = 0x128;
__device__ __forceinline__ float ror8(float x) {
  return __builtin_bit_cast(float, __builtin_amdgcn_mov_dpp(__builtin_bit_cast(int, x), ROR8, 0xf, 0xf, true));
}
__device__ __forceinline__ float max3(float a, float b, float c) { return fmaxf(fmaxf(a, b), c); }
__device__ __forceinline__ float xrow_max(float x) {
  auto s = __builtin_amdgcn_permlane16_swap(__float_as_uint(x), __float_as_uint(x), false, false);
  x = fmaxf(__uint_as_float(s[0]), __uint_as_float(s[1]));
  auto t = __builtin_amdgcn_permlane32_swap(__float_as_uint(x), __float_as_uint(x), false, false);
  return fmaxf(__uint_as_float(t[0]), __uint_as_float(t[1]));
}
__device__ __forceinline__ float xrow_sum(float x) {
  auto s = __builtin_amdgcn_permlane16_swap(__float_as_uint(x), __float_as_uint(x), false, false);
  x = __uint_as_float(s[0]) + __uint_as_float(s[1]);
  auto t = __builtin_amdgcn_permlane32_swap(__float_as_uint(x), __float_as_uint(x), false, false);
  return __uint_as_float(t[0]) + __uint_as_float(t[1]);
}
#define VSWAP(a, b) do { unsigned x_ = (a), y_ = (b); asm("v_swap_b32 %0, %1" : "+v"(x_), "+v"(y_)); (a) = x_; (b) = y_; } while (0)
__device__ __forceinline__ void transpose4(u32x4 (&v)[4]) {
  VSWAP(v[0][1], v[1][0]); VSWAP(v[0][2], v[2][0]); VSWAP(v[1][2], v[2][1]);
  VSWAP(v[0][3], v[3][0]); VSWAP(v[1][3], v[3][1]); VSWAP(v[2][3], v[3][2]);
  asm("s_nop 1" : "+v"(v[0]), "+v"(v[1]), "+v"(v[2]), "+v"(v[3]));
}
template <typename T> __device__ __forceinline__ void lds_st(LAS char* p, T v) { *reinterpret_cast<LAS T*>(p) = v; }

struct Epi {
  LAS char *ml_w, *acc_w, *l4, *g_r, *wg_w;
  unsigned a0, a1, a2, a3, a4, a5, a6, a7, t2;
  int out_off, lse_off;
  unsigned sel_lo, sel_hi;
};

template <int N, bool FIRST, typename F>
__device__ __forceinline__ void do_round(const unsigned char* kc, const unsigned char* vc, const int (&phys)[N], int rem0, int remk0,
                                         int lane, int koff, int voff, u32x4 (&qf)[2], float& m_run, f32x4 (&acc)[5], Epi& ep,
                                         F&& init_epi, bool last, float& lse, bool& mine, u32x2 lens, int w, int bid,
                                         __amdgpu_buffer_rsrc_t wr) {
  const int c = lane >> 4;
  const float c2 = 0.088388347648318440f * 1.4426950408889634f;
  const u32x4 ones = u32x4{0x3f803f80u, 0x3f803f80u, 0x3f803f80u, 0x3f803f80u};
  u32x4 kf[N][4], vf[N][4], pf[N];
  float x[N][4];
  f32x4 d[2];
  unsigned long long t1;
  unsigned tb, tx;
  int l0, l1;
#pragma unroll
  for (int st = 0; st < 2 * N + LA; ++st) {
    const int g = st - LA;
    if (g == 0 && FIRST) {
      init_epi();
      asm volatile("" : "+v"(ep.ml_w), "+v"(ep.acc_w), "+v"(ep.l4), "+v"(ep.g_r), "+v"(ep.wg_w), "+v"(ep.a0), "+v"(ep.a1), "+v"(ep.a2),
                   "+v"(ep.a3), "+v"(ep.a4), "+v"(ep.a5), "+v"(ep.a6), "+v"(ep.a7), "+v"(ep.t2), "+v"(ep.out_off), "+v"(ep.lse_off),
                   "+v"(ep.sel_lo), "+v"(ep.sel_hi));
      const unsigned qm = remk0 != rem0 ? 0u : ~0u;
      qf[0] &= qm;
      qf[1] &= qm;
    }
    if (g >= 0 && g < N) {
#pragma unroll
      for (int hh = 0; hh < 2; ++hh) d[hh] = mfma16(kf[g][2 * hh], qf[0], f32x4{0.f, 0.f, 0.f, 0.f});
#pragma unroll
      for (int hh = 0; hh < 2; ++hh) d[hh] = mfma16(kf[g][2 * hh + 1], qf[1], d[hh]);
      asm volatile("" : "+v"(d[0]), "+v"(d[1]));
      __builtin_amdgcn_sched_barrier(0);
    }
    if (st < 2 * N) {
      const int u = st < N ? st : st - N;
      if (FIRST && st >= 2) {
        if (st == 2) {
          asm volatile("s_waitcnt lgkmcnt(0)" : "+s"(t1), "+s"(lens), "+s"(bid));
          l0 = min(lens[0] - 1u, N * NSLOT * PAGE - 1u);
          l1 = min(lens[1] - 1u, N * NSLOT * PAGE - 1u);
          tb = (unsigned)t1 + TICK0;
          asm volatile("s_cmp_lg_u32 %3, 15\n\ts_cbranch_scc1 1f\n\tbuffer_store_dword %0, off, %1, %2 offset:%4 sc1\n1:"
                       :: "v"(tb), "s"(wr), "s"((bid & 7) * 4), "s"(w * 32 + (bid >> 3)), "i"(WS_T) : "scc");
        }
        if (st == 3 + LA) {
          int dt = (int)(tx - tb);
          dt = (unsigned)(dt + 200) < 400u ? dt : 0;
          dt += __builtin_amdgcn_mov_dpp(dt, 0xb1, 0xf, 0xf, true);
          dt += __builtin_amdgcn_mov_dpp(dt, 0x4e, 0xf, 0xf, true);
          dt += __builtin_amdgcn_mov_dpp(dt, 0x141, 0xf, 0xf, true);
          tb += __builtin_amdgcn_readfirstlane(dt) >> 3;
        }
        const int e = (u * NSLOT + (w >> 1) * TEAM) * PAGE;
        const int pos = (st < N ? 0 : l0 + l1) + min(l0, e + (w & 1) * TEAM * PAGE) + min(l1, e);
        asm volatile("1:\n\ts_memrealtime vcc\n\ts_waitcnt lgkmcnt(0)\n\ts_sub_u32 vcc_lo, vcc_lo, %0\n\ts_cmp_ge_i32 vcc_lo, 0\n\t"
                     "s_cbranch_scc1 2f\n\ts_sleep 2\n\ts_branch 1b\n2:"
                     :: "s"(tb + (pos * TICKS >> 13)) : "scc", "vcc");
        __builtin_amdgcn_sched_barrier(0);
        if (st == 3) tx = __builtin_amdgcn_raw_buffer_load_b32(wr, WS_T + (lane & 7) * 4, 0, AUX_SC1);
        if (st == 2 * N - 1) {
          asm volatile("s_cmp_lg_u32 %3, 0\n\ts_cbranch_scc1 1f\n\tbuffer_atomic_or_x2 %0, %1, %2, 0 offen sc1\n1:"
                       :: "v"(u32x2{0u, 0u}), "v"(ep.lse_off), "s"(wr), "s"(w) : "scc");
          __builtin_amdgcn_sched_barrier(0);
        }
      }
      const int rem = (st < N ? remk0 : rem0) - u * NSLOT * PAGE;
      const int nrec = (rem > 0 ? rem : 0) * TOK_BYTES;
      const size_t off = (size_t)(unsigned)phys[u] * PAGE_BYTES;
      __amdgpu_buffer_rsrc_t r = __builtin_amdgcn_make_buffer_rsrc((void*)((st < N ? kc : vc) + off), 0, nrec, RSRC3);
#pragma unroll
      for (int s = 0; s < 4; ++s) {
        if (st < N) kf[u][s] = __builtin_amdgcn_raw_buffer_load_b128(r, koff + (s & 1) * 128 + (s >> 1) * TOK_BYTES, 0, AUX_NT);
        else vf[u][s] = __builtin_amdgcn_raw_buffer_load_b128(r, voff + TOK_BYTES * s, 0, AUX_NT);
      }
      __builtin_amdgcn_sched_barrier(0);
      if (FIRST && st == 0) asm volatile("s_memrealtime %0" : "=s"(t1));
      if (FIRST && st == 0) __builtin_amdgcn_s_barrier();
    }
    if (g >= 0 && g < N) {
#pragma unroll
      for (int hh = 0; hh < 2; ++hh)
#pragma unroll
        for (int a = 0; a < 2; ++a) {
          const float t = d[hh][2 * a] + ror8(d[hh][2 * a + 1]);
          x[g][2 * a + hh] = __builtin_bit_cast(
              float, __builtin_amdgcn_update_dpp(__builtin_bit_cast(int, t), __builtin_bit_cast(int, t), ROR8, 0xf, 0xc, false));
        }
      asm volatile("" : "+v"(x[g][0]), "+v"(x[g][1]), "+v"(x[g][2]), "+v"(x[g][3]));
      __builtin_amdgcn_sched_barrier(0);
    }
    if (g == N - 1) {
      if (rem0 - (N - 1) * NSLOT * PAGE < PAGE) {
#pragma unroll
        for (int u = 0; u < N; ++u)
#pragma unroll
          for (int j = 0; j < 4; ++j) x[u][j] = (4 * c + j < rem0 - u * NSLOT * PAGE) ? x[u][j] : -INFINITY;
      }
      float mx = -INFINITY;
#pragma unroll
      for (int u = 0; u < N; ++u) mx = max3(max3(mx, x[u][0], x[u][1]), x[u][2], x[u][3]);
      const float m_new = fmaxf(m_run, xrow_max(mx) * c2);
      if (!FIRST) {
        const float alpha = __builtin_amdgcn_exp2f(m_run - m_new);
#pragma unroll
        for (int k = 0; k < 5; ++k) acc[k] *= alpha;
      }
      m_run = m_new;
#pragma unroll
      for (int u = 0; u < N; ++u) {
#pragma unroll
        for (int j = 0; j < 4; j += 2) {
          const unsigned pk = cvtpk(__builtin_amdgcn_exp2f(fmaf(x[u][j], c2, -m_new)), __builtin_amdgcn_exp2f(fmaf(x[u][j + 1], c2, -m_new)));
          pf[u][j] = __builtin_amdgcn_perm(0u, pk, ep.sel_lo);
          pf[u][j + 1] = __builtin_amdgcn_perm(0u, pk, ep.sel_hi);
        }
        acc[4] = mfma16(ones, pf[u], acc[4]);
      }
      asm volatile("" : "+v"(acc[4]));
      __builtin_amdgcn_sched_barrier(0);
    }
    if (g == N) {
      lds_st<float>(ep.ml_w, m_run);
      lds_st<float>(ep.ml_w + (L_L - L_M), acc[4][0]);
      unsigned old;
      float mj, lj;
      asm volatile("ds_add_rtn_u32 %0, %3, %4 offset:%5\n\tds_read_b32 %1, %3 offset:%6\n\tds_read_b32 %2, %3 offset:%7\n\ts_waitcnt lgkmcnt(0)"
                   : "=&v"(old), "=&v"(mj), "=&v"(lj)
                   : "v"(ep.l4), "v"(last ? 1u : 0u), "i"(L_CNT), "i"(L_M), "i"(L_L)
                   : "memory");
      mine = last && __builtin_amdgcn_readfirstlane(old) == NWAVE - 1;
      const float M = xrow_max(mj);
      const float wt = __builtin_amdgcn_exp2f(mj - M);
      const float L = xrow_sum(wt * lj);
      lds_st<float>(ep.wg_w + (mine ? 0 : 256), L > 0.f ? wt * __builtin_amdgcn_rcpf(L) : 0.f);
      lse = M + __builtin_amdgcn_logf(L);
      asm volatile("" : "+v"(lse));
      __builtin_amdgcn_sched_barrier(0);
    }
    if (g >= N) {
      const int u = g - N;
      transpose4(vf[u]);
#pragma unroll
      for (int k = 0; k < 4; ++k) acc[k] = mfma16(vf[u][k], pf[u], acc[k]);
      __builtin_amdgcn_sched_barrier(0);
    }
  }
}

__global__ void __launch_bounds__(NTHR, 2)
attn_decode(const int* bt_, const int* lens_, unsigned char* ws_, const unsigned short* q_, const unsigned char* kc_,
            const unsigned char* vc_, unsigned short* out_, const void* pad_) {
  __shared__ __attribute__((aligned(16))) char lds_[L_END];
  LAS char* lds = (LAS char*)lds_;
  u32x16 ka;
  asm volatile("s_load_dwordx16 %0, %1, 0x0" : "=s"(ka) : "s"(__builtin_amdgcn_kernarg_segment_ptr()));
  unsigned cw = (threadIdx.x & 63) * 128;
  {
    unsigned long long pc;
    asm volatile("s_getpc_b64 %1\n.Lattn_pc:\n\tv_min_u32 %0, .Lattn_end-.Lattn_pc, %0\n\tglobal_load_dword %0, %0, %1" : "+v"(cw), "=&s"(pc));
  }
  const int bid = blockIdx.x;
  const int sw = (bid >> 3) & 1, team = (bid & 1) * 2 + sw, rank = ((bid >> 1) & 3) | ((bid >> 4) << 2);
  const int w = __builtin_amdgcn_readfirstlane(threadIdx.x >> 6);
  const int b = w & 1, kvh = team ^ b;
  const int slot = (w >> 1) * TEAM + (rank ^ (b * (TEAM - 1)));
  const int bt_off = (b * NBPS + slot) * 4;
  asm volatile("s_waitcnt lgkmcnt(0)" : "+s"(ka) : "s"(bt_off), "s"(WS_EPOCH + team * 256));
#define KPTR(n) (((unsigned long long)ka[2 * (n) + 1] << 32) | ka[2 * (n)])
  int phys[NPW];
  u32x2 lens;
  unsigned epoch;
  static_assert(NPW == 5 && NSLOT * 4 == 0x400, "block-table offsets below");
  asm volatile(
      "s_load_dword %0, %7, %10 offset:0x0\n\ts_load_dwordx2 %5, %8, 0x0\n\ts_load_dword %1, %7, %10 offset:0x400\n\t"
      "s_load_dword %2, %7, %10 offset:0x800\n\ts_load_dword %3, %7, %10 offset:0xc00\n\ts_load_dword %4, %7, %10 offset:0x1000\n\t"
      "s_load_dword %6, %9, %11 offset:0x0"
      : "=&s"(phys[0]), "=&s"(phys[1]), "=&s"(phys[2]), "=&s"(phys[3]), "=&s"(phys[4]), "=&s"(lens), "=&s"(epoch)
      : "s"(KPTR(0)), "s"(KPTR(1)), "s"(KPTR(2)), "s"(bt_off), "s"(WS_EPOCH + team * 256));
  int tid = threadIdx.x;
  asm volatile("" : "+v"(tid));
  const int lane = tid & 63, c = lane >> 4, i = lane & 15;
  lds_st<unsigned>(lds + L_CNT + lane * 4, 0u);
  lds_st<unsigned>(lds + L_LS + 4 + lane * 8, 0u);
  const unsigned char* kc = (const unsigned char*)KPTR(4);
  const unsigned char* vc = (const unsigned char*)KPTR(5);
  u32x4 qf[2];
  {
    __amdgpu_buffer_rsrc_t qr = __builtin_amdgcn_make_buffer_rsrc((void*)KPTR(3), 0, NB * HQ * HD * 2, RSRC3);
#pragma unroll
    for (int s = 0; s < 2; ++s)
      qf[s] = __builtin_amdgcn_raw_buffer_load_b128(qr, (i & 7) * (HD * 2) + c * 32 + (i >> 3) * 16 + s * 128, (b * HQ + kvh * GQ) * HD * 2, 0);
  }
  const int koff = (i >> 1) * (2 * TOK_BYTES) + c * 32 + (i & 1) * 16 + kvh * (HD * 2);
  const int voff = c * 4 * TOK_BYTES + i * 16 + kvh * (HD * 2);
  __builtin_amdgcn_sched_barrier(0);
  asm volatile("s_waitcnt lgkmcnt(0)"
               : "+s"(phys[0]), "+s"(phys[1]), "+s"(phys[2]), "+s"(phys[3]), "+s"(phys[4]), "+s"(lens), "+s"(epoch)
               : "v"(koff), "v"(voff));
  epoch += 0x4001u;
  const unsigned tag = (epoch * 0x9E3779B1u + 0x7F4A7C15u) | 1u;
  float m_run = -1e30f;
  f32x4 acc[5];
#pragma unroll
  for (int k = 0; k < 5; ++k) acc[k] = f32x4{0.f, 0.f, 0.f, 0.f};

  int len = b ? lens[1] : lens[0];
  const bool uni = len <= 0;
  len = uni ? SKV : (len < SKV ? len : SKV);
  const int rem0 = len - slot * PAGE, remk0 = (uni ? 0 : len) - slot * PAGE;

  __amdgpu_buffer_rsrc_t wr = __builtin_amdgcn_make_buffer_rsrc((void*)KPTR(2), 0, WS_END, RSRC3);
  Epi ep;
  auto init_epi = [&]() {
    const int p8 = lane >> 3, e = lane & 7;
    ep.ml_w = lds + L_M + ((w >> 1) * 16 + b * 8 + e) * 4;
    ep.wg_w = lds + L_WG + i * 16 + c * 4;
    ep.acc_w = lds + w * L_SW + lane * 16;
    ep.l4 = lds + lane * 4;
    ep.g_r = lds + L_G + lane * 144 + w * 16;
    const int hd = (w >> 1) & 1, ch = w & 1, cons = (hd * 8 + p8) * 2 + ch;
    const int p0 = (w & 3) * 16 + p8, p1 = p0 + 8;
    const bool mg = w < 4;
    const unsigned lb = (unsigned)(size_t)lds;
    const int pa = mg ? rank : p0, ca = mg ? cons : rank;
    const unsigned o0 = WS_O + ((team * TEAM + pa) * NCONS + ((ca + pa) & (NCONS - 1))) * 128 + e * 16;
    const unsigned o1 = WS_O + ((team * TEAM + p1) * NCONS + ((rank + p1) & (NCONS - 1))) * 128 + e * 16;
    ep.a0 = mg ? lb + L_WG + (hd * 8 + p8) * 16 : WS_F + ((team * 16 + (rank >> 1)) * TEAM + (w & 3) * 16 + i) * 8;
    ep.a1 = (!mg && p0 == rank) ? WS_END : o0;
    ep.a2 = mg ? lb + hd * L_SW + (e >> 1) * L_SK + (ch * 32 + (e & 1) * 8 + p8) * 16 : (p1 == rank ? WS_END : o1);
    ep.a3 = lb + L_G + (mg ? (cons == rank ? rank : TEAM) : (p0 == rank ? TEAM : p0)) * 144 + e * 16;
    ep.a4 = lb + L_G + (p1 == rank ? TEAM : p1) * 144 + e * 16;
    ep.a5 = mg ? 0x4b002000u : (p0 == rank ? 0u : 0x3c000u);
    ep.a6 = mg ? ((rank | lane) == 0 ? WS_EPOCH + team * 256 : WS_END) : (p1 == rank ? 0u : 0x3c000u);
    ep.a7 = lb + L_LS + (mg ? lane : (w & 3) * 16 + i) * 8;
    ep.t2 = (epoch + (e << 15)) & 0x3c000u;
    const int ob = rank >> 4;
    ep.out_off = (e == 0) ? ((ob * HQ + (team ^ ob) * GQ + ((rank >> 1) & 7)) * HD + (rank & 1) * 64 + p8 * 8 + w) * 2 : NB * HQ * HD * 2;
    ep.lse_off = lane < 16 ? WS_F + ((team * 16 + lane) * TEAM + rank) * 8 : WS_END;
    ep.sel_lo = (lane & 8) ? 0x01000c0cu : 0x0c0c0100u;
    ep.sel_hi = (lane & 8) ? 0x03020c0cu : 0x0c0c0302u;
  };

  float lse;
  bool mine;
  do_round<NPW, true>(kc, vc, phys, rem0, remk0, lane, koff, voff, qf, m_run, acc, ep, init_epi, rem0 <= NPW * NSLOT * PAGE, lse, mine,
                      lens, w, bid, wr);
  if (__builtin_expect(rem0 > NPW * NSLOT * PAGE, 0)) {
    constexpr int N2 = NBPS / NSLOT - NPW;
    int phys2[N2];
#pragma unroll
    for (int u = 0; u < N2; ++u) phys2[u] = ((const __attribute__((address_space(4))) int*)KPTR(0))[b * NBPS + slot + (NPW + u) * NSLOT];
    do_round<N2, false>(kc, vc, phys2, rem0 - NPW * NSLOT * PAGE, remk0 - NPW * NSLOT * PAGE, lane, koff, voff, qf, m_run, acc, ep,
                        init_epi, true, lse, mine, lens, w, bid, wr);
  }
  asm volatile("" ::"v"(cw));

#pragma unroll
  for (int k = 0; k < 4; ++k) lds_st<f32x4>(ep.acc_w + k * L_SK, acc[k]);

#define DPPF " row_mask:0xf bank_mask:0xf\n\t"
#define WAVE_RED(op)                                             \
  op "_dpp v13, v12, v12 quad_perm:[1,0,3,2]" DPPF "s_nop 1\n\t" \
  op "_dpp v13, v13, v13 quad_perm:[2,3,0,1]" DPPF "s_nop 1\n\t" \
  op "_dpp v13, v13, v13 row_half_mirror" DPPF "s_nop 1\n\t"     \
  op "_dpp v14, v13, v13 row_ror:8" DPPF                         \
  op "_dpp v13, v13, v13 row_ror:8" DPPF "s_nop 1\n\t"           \
  "v_permlane16_swap_b32 v13, v14\n\t"                           \
  op " v15, v13, v14\n\t" op " v13, v13, v14\n\ts_nop 1\n\t"     \
  "v_permlane32_swap_b32 v13, v15\n\t"                           \
  op " v13, v13, v15\n\t"
#define POLL(lo, hi) "v_mov_b32 v" #lo ", 0\n\tv_mov_b32 v" #hi ", 0\n\tbuffer_atomic_or_x2 v[" #lo ":" #hi "], %[a0], %[wr], 0 offen sc0 sc1\n\t"
#define SEEN(lo, hi) "v_cmp_ne_u32 vcc, %[tag], v" #hi "\n\tds_write_b64 %[a7], v[" #lo ":" #hi "]\n\t"
  unsigned spins = 1u << 20, spins2 = 1u << 20;
  __amdgpu_buffer_rsrc_t orr = __builtin_amdgcn_make_buffer_rsrc((void*)KPTR(6), 0, NB * HQ * HD * 2, RSRC3);
  asm volatile(
      "s_cmp_gt_u32 %[w], 3\n\t"
      "s_waitcnt lgkmcnt(0)\n\t"
      "s_barrier\n\t"
      "buffer_atomic_swap_x2 %[lg], %[lo], %[wr], 0 offen sc1\n\t"
      "s_cbranch_scc1 10f\n\t"
      "ds_read_b128 v[4:7], %[a0]\n\t"
      "ds_read_b128 v[8:11], %[a2]\n\t"
      "ds_read_b128 v[12:15], %[a2] offset:256\n\t"
      "ds_read_b128 v[16:19], %[a2] offset:%[o1]\n\t"
      "ds_read_b128 v[20:23], %[a2] offset:%[o1]+256\n\t"
      "ds_read_b128 v[24:27], %[a2] offset:2*%[o1]\n\t"
      "ds_read_b128 v[28:31], %[a2] offset:2*%[o1]+256\n\t"
      "ds_read_b128 v[32:35], %[a2] offset:3*%[o1]\n\t"
      "ds_read_b128 v[36:39], %[a2] offset:3*%[o1]+256\n\t"
      "s_waitcnt lgkmcnt(6)\n\t"
      "v_pk_mul_f32 v[8:9], v[8:9], v[4:5] op_sel_hi:[1,0]\n\t"
      "v_pk_mul_f32 v[10:11], v[10:11], v[4:5] op_sel_hi:[1,0]\n\t"
      "v_pk_mul_f32 v[12:13], v[12:13], v[4:5] op_sel_hi:[1,0]\n\t"
      "v_pk_mul_f32 v[14:15], v[14:15], v[4:5] op_sel_hi:[1,0]\n\t"
      "s_waitcnt lgkmcnt(4)\n\t"
      "v_pk_fma_f32 v[8:9], v[16:17], v[4:5], v[8:9] op_sel:[0,1,0]\n\t"
      "v_pk_fma_f32 v[10:11], v[18:19], v[4:5], v[10:11] op_sel:[0,1,0]\n\t"
      "v_pk_fma_f32 v[12:13], v[20:21], v[4:5], v[12:13] op_sel:[0,1,0]\n\t"
      "v_pk_fma_f32 v[14:15], v[22:23], v[4:5], v[14:15] op_sel:[0,1,0]\n\t"
      "s_waitcnt lgkmcnt(2)\n\t"
      "v_pk_fma_f32 v[8:9], v[24:25], v[6:7], v[8:9] op_sel_hi:[1,0,1]\n\t"
      "v_pk_fma_f32 v[10:11], v[26:27], v[6:7], v[10:11] op_sel_hi:[1,0,1]\n\t"
      "v_pk_fma_f32 v[12:13], v[28:29], v[6:7], v[12:13] op_sel_hi:[1,0,1]\n\t"
      "v_pk_fma_f32 v[14:15], v[30:31], v[6:7], v[14:15] op_sel_hi:[1,0,1]\n\t"
      "s_waitcnt lgkmcnt(0)\n\t"
      "v_pk_fma_f32 v[8:9], v[32:33], v[6:7], v[8:9] op_sel:[0,1,0]\n\t"
      "v_pk_fma_f32 v[10:11], v[34:35], v[6:7], v[10:11] op_sel:[0,1,0]\n\t"
      "v_pk_fma_f32 v[12:13], v[36:37], v[6:7], v[12:13] op_sel:[0,1,0]\n\t"
      "v_pk_fma_f32 v[14:15], v[38:39], v[6:7], v[14:15] op_sel:[0,1,0]\n\t"
      "v_max3_f32 v16, |v8|, |v9|, |v10|\n\t"
      "v_max3_f32 v17, |v11|, |v12|, |v13|\n\t"
      "v_max3_f32 v18, |v14|, |v15|, %[emin]\n\t"
      "v_max3_f32 v16, v16, v17, v18\n\t"
      "v_mov_b32 v20, %[a5]\n\t"
      "v_and_b32 v16, 0x7f800000, v16\n\t"
      "v_sub_u32 v18, 0x84800000, v16\n\t"
      "v_lshrrev_b32 v17, 9, v16\n\t"
      "v_lshrrev_b32 v16, 13, v16\n\t"
      "v_pk_fma_f32 v[8:9], v[8:9], v[18:19], v[20:21] op_sel_hi:[1,0,0]\n\t"
      "v_pk_fma_f32 v[10:11], v[10:11], v[18:19], v[20:21] op_sel_hi:[1,0,0]\n\t"
      "v_pk_fma_f32 v[12:13], v[12:13], v[18:19], v[20:21] op_sel_hi:[1,0,0]\n\t"
      "v_pk_fma_f32 v[14:15], v[14:15], v[18:19], v[20:21] op_sel_hi:[1,0,0]\n\t"
      "v_and_b32 v17, 0x3c000, v17\n\t"
      "v_lshl_or_b32 v4, v9, 18, %[t2]\n\t"
      "v_lshl_or_b32 v5, v11, 18, v17\n\t"
      "v_lshl_or_b32 v6, v13, 18, %[t2]\n\t"
      "v_lshl_or_b32 v7, v15, 18, v16\n\t"
      "v_bfi_b32 v4, %[m14], v8, v4\n\t"
      "v_bfi_b32 v5, %[m14], v10, v5\n\t"
      "v_bfi_b32 v6, %[m14], v12, v6\n\t"
      "v_bfi_b32 v7, %[m14], v14, v7\n\t"
      "buffer_store_dwordx4 v[4:7], %[a1], %[wr], 0 offen sc0 sc1\n\t"
      "s_cmp_gt_u32 %[rk], %[nc]\n\t"
      "s_cbranch_scc1 30f\n\t"
      "ds_write_b128 %[a3], v[4:7]\n\t"
      "s_cmp_lg_u32 %[w], 0\n\t"
      "s_cbranch_scc1 20f\n"
      "1:\n\t"
      "ds_read_b64 v[12:13], %[a7]\n\t"
      "s_sub_u32 %[sp], %[sp], 1\n\t"
      "s_waitcnt lgkmcnt(0)\n\t"
      "s_cbranch_scc1 2f\n\t"
      "v_cmp_ne_u32 vcc, %[tag], v13\n\t"
      "s_sleep 1\n\t"
      "s_cbranch_vccnz 1b\n"
      "2:\n\t"
      "v_mov_b32 v16, %[ep]\n\t"
      "buffer_store_dword v16, %[a6], %[wr], 0 offen sc1\n\t"
      WAVE_RED("v_max_f32")
      "v_sub_f32 v12, v12, v13\n\t"
      "v_exp_f32 v12, v12\n\t"
      "s_nop 1\n\t"
      WAVE_RED("v_add_f32")
      "v_rcp_f32 v13, v13\n\t"
      "v_mul_f32 v12, 0x3a000000, v12\n\t"
      "v_mul_f32 v12, v12, v13\n\t"
      "ds_write_b32 %[l4], v12 offset:%[w2]\n\t"
      "s_branch 20f\n"
      "10:\n\t"
      "s_cmp_gt_u32 %[rk], %[nc]\n\t"
      "s_cbranch_scc1 30f\n\t"
      "s_mov_b64 exec, 0xffff\n\t"
      POLL(12, 13) "s_sleep 4\n\t"
      POLL(14, 15) "s_sleep 4\n\t"
      POLL(16, 17) "s_sleep 4\n"
      "11:\n\t"
      POLL(18, 19) "s_waitcnt vmcnt(3)\n\t" SEEN(12, 13) "s_cbranch_vccz 12f\n\t"
      POLL(12, 13) "s_waitcnt vmcnt(3)\n\t" SEEN(14, 15) "s_cbranch_vccz 12f\n\t"
      POLL(14, 15) "s_waitcnt vmcnt(3)\n\t" SEEN(16, 17) "s_cbranch_vccz 12f\n\t"
      POLL(16, 17) "s_sub_u32 %[sp], %[sp], 1\n\ts_waitcnt vmcnt(3)\n\ts_cbranch_scc1 12f\n\t" SEEN(18, 19) "s_cbranch_vccnz 11b\n"
      "12:\n\t"
      "s_mov_b64 exec, -1\n\t"
      "13:\n\t"
      "buffer_load_dwordx4 v[4:7], %[a1], %[wr], 0 offen sc1\n\t"
      "buffer_load_dwordx4 v[8:11], %[a2], %[wr], 0 offen sc1\n\t"
      "s_sub_u32 %[sq], %[sq], 1\n\t"
      "s_waitcnt vmcnt(1)\n\t"
      "v_bitop3_b32 v20, v4, v6, %[t2] bitop3:0x7e\n\t"
      "ds_write_b128 %[a3], v[4:7]\n\t"
      "v_and_b32 v20, v20, %[a5]\n\t"
      "s_waitcnt vmcnt(0)\n\t"
      "ds_write_b128 %[a4], v[8:11]\n\t"
      "v_bitop3_b32 v21, v8, v10, %[t2] bitop3:0x7e\n\t"
      "v_bitop3_b32 v20, v21, v20, %[a6] bitop3:0xec\n\t"
      "s_cbranch_scc1 20f\n\t"
      "v_cmp_ne_u32 vcc, 0, v20\n\t"
      "s_cbranch_vccnz 13b\n\t"
      "20:\n\t"
      "s_waitcnt lgkmcnt(0)\n\t"
      "s_barrier\n\t"
      "ds_read_b128 v[4:7], %[grd]\n\t"
      "ds_read_b32 v12, %[l4] offset:%[w2]\n\t"
      "v_mov_b32 v16, 0x4b000000\n\t"
      "v_mov_b32 v14, 0xcb002000\n\t"
      "s_waitcnt lgkmcnt(1)\n\t"
      "v_bfe_u32 v13, v5, 14, 4\n\t"
      "v_bfe_u32 v15, v7, 14, 4\n\t"
      "v_and_or_b32 v8, v4, %[m14], v16\n\t"
      "v_and_or_b32 v9, v5, %[m14], v16\n\t"
      "v_lshl_or_b32 v13, v15, 4, v13\n\t"
      "v_and_or_b32 v10, v6, %[m14], v16\n\t"
      "v_and_or_b32 v11, v7, %[m14], v16\n\t"
      "v_lshlrev_b32 v13, 23, v13\n\t"
      "v_alignbit_b32 v4, %[mag], v4, 18\n\t"
      "v_alignbit_b32 v5, %[mag], v5, 18\n\t"
      "v_alignbit_b32 v6, %[mag], v6, 18\n\t"
      "v_alignbit_b32 v7, %[mag], v7, 18\n\t"
      "s_waitcnt lgkmcnt(0)\n\t"
      "v_mul_f32 v12, v12, v13\n\t"
      "v_pk_add_f32 v[8:9], v[8:9], v[14:15] op_sel_hi:[1,0]\n\t"
      "v_pk_add_f32 v[10:11], v[10:11], v[14:15] op_sel_hi:[1,0]\n\t"
      "v_pk_add_f32 v[4:5], v[4:5], v[14:15] op_sel_hi:[1,0]\n\t"
      "v_pk_add_f32 v[6:7], v[6:7], v[14:15] op_sel_hi:[1,0]\n\t"
      "v_pk_mul_f32 v[8:9], v[8:9], v[12:13] op_sel_hi:[1,0]\n\t"
      "v_pk_mul_f32 v[10:11], v[10:11], v[12:13] op_sel_hi:[1,0]\n\t"
      "v_pk_mul_f32 v[4:5], v[4:5], v[12:13] op_sel_hi:[1,0]\n\t"
      "v_pk_mul_f32 v[6:7], v[6:7], v[12:13] op_sel_hi:[1,0]\n\t"
      "v_permlane32_swap_b32 v8, v10\n\t"
      "v_permlane32_swap_b32 v9, v11\n\t"
      "v_permlane32_swap_b32 v4, v6\n\t"
      "v_permlane32_swap_b32 v5, v7\n\t"
      "v_pk_add_f32 v[8:9], v[8:9], v[10:11]\n\t"
      "v_pk_add_f32 v[4:5], v[4:5], v[6:7]\n\t"
      "s_nop 0\n\t"
      "v_permlane16_swap_b32 v8, v9\n\t"
      "v_permlane16_swap_b32 v4, v5\n\t"
      "v_add_f32 v8, v8, v9\n\t"
      "v_add_f32 v4, v4, v5\n\t"
      "s_nop 0\n\t"
      "v_add_f32_dpp v8, v8, v8 row_ror:8 row_mask:0xf bank_mask:0x3\n\t"
      "v_add_f32_dpp v8, v4, v4 row_ror:8 row_mask:0xf bank_mask:0xc\n\t"
      "s_nop 1\n\t"
      "v_add_f32_dpp v8, v8, v8 row_half_mirror" DPPF
      "s_nop 1\n\t"
      "v_add_f32_dpp v8, v8, v8 quad_perm:[2,3,0,1]" DPPF
      "s_nop 1\n\t"
      "v_add_f32_dpp v8, v8, v8 quad_perm:[1,0,3,2]" DPPF
      "v_cvt_pk_bf16_f32 v8, v8, v8\n\t"
      "buffer_store_short v8, %[oo], %[orr], 0 offen\n\t"
      "30:\n"
      ".Lattn_end:"
      : [sp] "+s"(spins), [sq] "+s"(spins2)
      : [wr] "s"(wr), [orr] "s"(orr), [lg] "v"(u32x2{__float_as_uint(lse), tag}), [lo] "v"(mine ? ep.lse_off : WS_END), [w] "s"(w),
        [rk] "s"(rank), [nc] "i"(NCONS - 1), [a0] "v"(ep.a0), [a1] "v"(ep.a1), [a2] "v"(ep.a2), [a3] "v"(ep.a3), [a4] "v"(ep.a4),
        [a5] "v"(ep.a5), [a6] "v"(ep.a6), [a7] "v"(ep.a7), [l4] "v"(ep.l4), [grd] "v"(ep.g_r), [oo] "v"(ep.out_off), [w2] "i"(L_W2),
        [o1] "i"(2 * L_SW), [tag] "s"(tag), [ep] "s"(epoch), [t2] "v"(ep.t2), [m14] "s"(0x3fffu), [mag] "s"(0x4b000000u >> 14),
        [emin] "s"(0x0a000000u)
      : "memory", "vcc", "scc", "v4", "v5", "v6", "v7", "v8", "v9", "v10", "v11", "v12", "v13", "v14", "v15", "v16", "v17", "v18", "v19",
        "v20", "v21", "v22", "v23", "v24", "v25", "v26", "v27", "v28", "v29", "v30", "v31", "v32", "v33", "v34", "v35", "v36", "v37",
        "v38", "v39");
}

extern "C" void kernel_launch(void* const* d_in, const int* in_sizes, int n_in, void* d_out, int out_size, void* d_ws,
                              size_t ws_size, hipStream_t stream) {
  attn_decode<<<NTEAM * TEAM, NTHR, 0, stream>>>((const int*)d_in[3], (const int*)d_in[4], (unsigned char*)d_ws,
                                                 (const unsigned short*)d_in[0], (const unsigned char*)d_in[1],
                                                 (const unsigned char*)d_in[2], (unsigned short*)d_out, d_ws);
}
```

```cpp
#include <hip/hip_runtime.h>
#include <stdint.h>

typedef __attribute__((ext_vector_type(2))) unsigned u32x2;
typedef __attribute__((ext_vector_type(4))) unsigned u32x4;
typedef __attribute__((ext_vector_type(16))) unsigned u32x16;
typedef __attribute__((ext_vector_type(2))) float f32x2;
typedef __attribute__((ext_vector_type(4))) float f32x4;
typedef __attribute__((ext_vector_type(2))) __bf16 bf16x2;
typedef __attribute__((ext_vector_type(8))) __bf16 bf16x8;
#define LAS __attribute__((address_space(3)))

constexpr int NB = 2, HQ = 32, HKV = 4, HD = 128, PAGE = 16, GQ = 8;
constexpr int NBPS = 2048, SKV = NBPS * PAGE;
constexpr int NWAVE = 8, NTHR = NWAVE * 64;
constexpr int NTEAM = 4, TEAM = 64, NCONS = 32;
constexpr int NSLOT = TEAM * 4;
constexpr int NPW = 5;
constexpr int LA = 3;
constexpr int TICKS = 118;
constexpr int TICK0 = 175 - 2 * TICKS;
constexpr int PAGE_BYTES = PAGE * HKV * HD * 2, TOK_BYTES = HKV * HD * 2;
constexpr int AUX_NT = 2, AUX_SC1 = 16, RSRC3 = 0x00020000;

constexpr int WS_EPOCH = 0, WS_T = 128, WS_F = 1024, WS_O = WS_F + NTEAM * 16 * TEAM * 8;
constexpr int WS_END = WS_O + NTEAM * TEAM * NCONS * 128;

constexpr int L_SK = 1088, L_SW = 4 * L_SK;
constexpr int L_M = NWAVE * L_SW, L_L = L_M + 256, L_WG = L_L + 256, L_W2 = L_WG + 512, L_CNT = L_W2 + 256;
constexpr int L_LS = L_CNT + 256, L_G = L_LS + 512, L_END = L_G + (TEAM + 1) * 144;

__device__ __forceinline__ f32x4 mfma16(u32x4 a, u32x4 b, f32x4 c) {
  return __builtin_amdgcn_mfma_f32_16x16x32_bf16(__builtin_bit_cast(bf16x8, a), __builtin_bit_cast(bf16x8, b), c, 0, 0, 0);
}
__device__ __forceinline__ unsigned cvtpk(float lo, float hi) {
  return __builtin_bit_cast(unsigned, __builtin_convertvector(f32x2{lo, hi}, bf16x2));
}
constexpr int ROR8 = 0x128;
__device__ __forceinline__ float ror8(float x) {
  return __builtin_bit_cast(float, __builtin_amdgcn_mov_dpp(__builtin_bit_cast(int, x), ROR8, 0xf, 0xf, true));
}
__device__ __forceinline__ float max3(float a, float b, float c) { return fmaxf(fmaxf(a, b), c); }
__device__ __forceinline__ float xrow_max(float x) {
  auto s = __builtin_amdgcn_permlane16_swap(__float_as_uint(x), __float_as_uint(x), false, false);
  x = fmaxf(__uint_as_float(s[0]), __uint_as_float(s[1]));
  auto t = __builtin_amdgcn_permlane32_swap(__float_as_uint(x), __float_as_uint(x), false, false);
  return fmaxf(__uint_as_float(t[0]), __uint_as_float(t[1]));
}
__device__ __forceinline__ float xrow_sum(float x) {
  auto s = __builtin_amdgcn_permlane16_swap(__float_as_uint(x), __float_as_uint(x), false, false);
  x = __uint_as_float(s[0]) + __uint_as_float(s[1]);
  auto t = __builtin_amdgcn_permlane32_swap(__float_as_uint(x), __float_as_uint(x), false, false);
  return __uint_as_float(t[0]) + __uint_as_float(t[1]);
}
#define VSWAP(a, b) do { unsigned x_ = (a), y_ = (b); asm("v_swap_b32 %0, %1" : "+v"(x_), "+v"(y_)); (a) = x_; (b) = y_; } while (0)
__device__ __forceinline__ void transpose4(u32x4 (&v)[4]) {
  VSWAP(v[0][1], v[1][0]); VSWAP(v[0][2], v[2][0]); VSWAP(v[1][2], v[2][1]);
  VSWAP(v[0][3], v[3][0]); VSWAP(v[1][3], v[3][1]); VSWAP(v[2][3], v[3][2]);
  asm("s_nop 1" : "+v"(v[0]), "+v"(v[1]), "+v"(v[2]), "+v"(v[3]));
}
template <typename T> __device__ __forceinline__ void lds_st(LAS char* p, T v) { *reinterpret_cast<LAS T*>(p) = v; }

struct Epi {
  LAS char *ml_w, *acc_w, *l4, *g_r, *wg_w;
  unsigned a0, a1, a2, a3, a4, a5, a6, a7, t2;
  int out_off, lse_off;
  unsigned sel_lo, sel_hi;
};

template <int N, bool FIRST, typename F, typename G>
__device__ __forceinline__ void do_round(const unsigned char* kc, const unsigned char* vc, const int (&phys)[N], int rem0, int remk0,
                                         int lane, int koff, int voff, u32x4 (&qf)[2], float& m_run, f32x4 (&acc)[5], Epi& ep,
                                         F&& init_epi, bool last, float& lse, bool& mine, u32x2 lens, int w, int bid,
                                         __amdgpu_buffer_rsrc_t wr, G&& load_rest) {
  const int c = lane >> 4;
  const float c2 = 0.088388347648318440f * 1.4426950408889634f;
  const u32x4 ones = u32x4{0x3f803f80u, 0x3f803f80u, 0x3f803f80u, 0x3f803f80u};
  u32x4 kf[N][4], vf[N][4], pf[N];
  float x[N][4];
  f32x4 d[2];
  unsigned long long t1;
  unsigned tb, tx;
  int l0, l1;
#pragma unroll
  for (int st = 0; st < 2 * N + LA; ++st) {
    const int g = st - LA;
    if (g == 0 && FIRST) {
      init_epi();
      asm volatile("" : "+v"(ep.ml_w), "+v"(ep.acc_w), "+v"(ep.l4), "+v"(ep.g_r), "+v"(ep.wg_w), "+v"(ep.a0), "+v"(ep.a1), "+v"(ep.a2),
                   "+v"(ep.a3), "+v"(ep.a4), "+v"(ep.a5), "+v"(ep.a6), "+v"(ep.a7), "+v"(ep.t2), "+v"(ep.out_off), "+v"(ep.lse_off),
                   "+v"(ep.sel_lo), "+v"(ep.sel_hi));
      const unsigned qm = remk0 != rem0 ? 0u : ~0u;
      qf[0] &= qm;
      qf[1] &= qm;
    }
    if (g >= 0 && g < N) {
#pragma unroll
      for (int hh = 0; hh < 2; ++hh) d[hh] = mfma16(kf[g][2 * hh], qf[0], f32x4{0.f, 0.f, 0.f, 0.f});
#pragma unroll
      for (int hh = 0; hh < 2; ++hh) d[hh] = mfma16(kf[g][2 * hh + 1], qf[1], d[hh]);
      asm volatile("" : "+v"(d[0]), "+v"(d[1]));
      __builtin_amdgcn_sched_barrier(0);
    }
    if (st < 2 * N) {
      const int u = st < N ? st : st - N;
      if (FIRST && st >= 2) {
        if (st == 2) {
          asm volatile("s_waitcnt lgkmcnt(0)" : "+s"(t1), "+s"(lens), "+s"(bid));
          l0 = min(lens[0] - 1u, N * NSLOT * PAGE - 1u);
          l1 = min(lens[1] - 1u, N * NSLOT * PAGE - 1u);
          tb = (unsigned)t1 + TICK0;
          asm volatile("s_cmp_lg_u32 %3, 15\n\ts_cbranch_scc1 1f\n\tbuffer_store_dword %0, off, %1, %2 offset:%4 sc1\n1:"
                       :: "v"(tb), "s"(wr), "s"((bid & 7) * 4), "s"(w * 32 + (bid >> 3)), "i"(WS_T) : "scc");
          load_rest();
        }
        if (st == 3 + LA) {
          int dt = (int)(tx - tb);
          dt = (unsigned)(dt + 200) < 400u ? dt : 0;
          dt += __builtin_amdgcn_mov_dpp(dt, 0xb1, 0xf, 0xf, true);
          dt += __builtin_amdgcn_mov_dpp(dt, 0x4e, 0xf, 0xf, true);
          dt += __builtin_amdgcn_mov_dpp(dt, 0x141, 0xf, 0xf, true);
          tb += __builtin_amdgcn_readfirstlane(dt) >> 3;
        }
        const int e = (u * NSLOT + (w >> 1) * TEAM) * PAGE;
        const int pos = (st < N ? 0 : l0 + l1) + min(l0, e + (w & 1) * TEAM * PAGE) + min(l1, e);
        asm volatile("1:\n\ts_memrealtime vcc\n\ts_waitcnt lgkmcnt(0)\n\ts_sub_u32 vcc_lo, vcc_lo, %0\n\ts_cmp_ge_i32 vcc_lo, 0\n\t"
                     "s_cbranch_scc1 2f\n\ts_sleep 2\n\ts_branch 1b\n2:"
                     :: "s"(tb + (pos * TICKS >> 13)) : "scc", "vcc");
        __builtin_amdgcn_sched_barrier(0);
        if (st == 3) tx = __builtin_amdgcn_raw_buffer_load_b32(wr, WS_T + (lane & 7) * 4, 0, AUX_SC1);
        if (st == 2 * N - 1) {
          asm volatile("s_cmp_lg_u32 %3, 0\n\ts_cbranch_scc1 1f\n\tbuffer_atomic_or_x2 %0, %1, %2, 0 offen sc1\n1:"
                       :: "v"(u32x2{0u, 0u}), "v"(ep.lse_off), "s"(wr), "s"(w) : "scc");
          __builtin_amdgcn_sched_barrier(0);
        }
      }
      const int rem = (st < N ? remk0 : rem0) - u * NSLOT * PAGE;
      const int nrec = (rem > 0 ? rem : 0) * TOK_BYTES;
      const size_t off = (size_t)(unsigned)phys[u] * PAGE_BYTES;
      __amdgpu_buffer_rsrc_t r = __builtin_amdgcn_make_buffer_rsrc((void*)((st < N ? kc : vc) + off), 0, nrec, RSRC3);
#pragma unroll
      for (int s = 0; s < 4; ++s) {
        if (st < N) kf[u][s] = __builtin_amdgcn_raw_buffer_load_b128(r, koff + (s & 1) * 128 + (s >> 1) * TOK_BYTES, 0, AUX_NT);
        else vf[u][s] = __builtin_amdgcn_raw_buffer_load_b128(r, voff + TOK_BYTES * s, 0, AUX_NT);
      }
      __builtin_amdgcn_sched_barrier(0);
      if (FIRST && st == 0) asm volatile("s_memrealtime %0" : "=s"(t1));
      if (FIRST && st == 0) __builtin_amdgcn_s_barrier();
    }
    if (g >= 0 && g < N) {
#pragma unroll
      for (int hh = 0; hh < 2; ++hh)
#pragma unroll
        for (int a = 0; a < 2; ++a) {
          const float t = d[hh][2 * a] + ror8(d[hh][2 * a + 1]);
          x[g][2 * a + hh] = __builtin_bit_cast(
              float, __builtin_amdgcn_update_dpp(__builtin_bit_cast(int, t), __builtin_bit_cast(int, t), ROR8, 0xf, 0xc, false));
        }
      asm volatile("" : "+v"(x[g][0]), "+v"(x[g][1]), "+v"(x[g][2]), "+v"(x[g][3]));
      __builtin_amdgcn_sched_barrier(0);
    }
    if (g == N - 1) {
      if (rem0 - (N - 1) * NSLOT * PAGE < PAGE) {
#pragma unroll
        for (int u = 0; u < N; ++u)
#pragma unroll
          for (int j = 0; j < 4; ++j) x[u][j] = (4 * c + j < rem0 - u * NSLOT * PAGE) ? x[u][j] : -INFINITY;
      }
      float mx = -INFINITY;
#pragma unroll
      for (int u = 0; u < N; ++u) mx = max3(max3(mx, x[u][0], x[u][1]), x[u][2], x[u][3]);
      const float m_new = fmaxf(m_run, xrow_max(mx) * c2);
      if (!FIRST) {
        const float alpha = __builtin_amdgcn_exp2f(m_run - m_new);
#pragma unroll
        for (int k = 0; k < 5; ++k) acc[k] *= alpha;
      }
      m_run = m_new;
#pragma unroll
      for (int u = 0; u < N; ++u) {
#pragma unroll
        for (int j = 0; j < 4; j += 2) {
          const unsigned pk = cvtpk(__builtin_amdgcn_exp2f(fmaf(x[u][j], c2, -m_new)), __builtin_amdgcn_exp2f(fmaf(x[u][j + 1], c2, -m_new)));
          pf[u][j] = __builtin_amdgcn_perm(0u, pk, ep.sel_lo);
          pf[u][j + 1] = __builtin_amdgcn_perm(0u, pk, ep.sel_hi);
        }
        acc[4] = mfma16(ones, pf[u], acc[4]);
      }
      asm volatile("" : "+v"(acc[4]));
      __builtin_amdgcn_sched_barrier(0);
    }
    if (g == N) {
      lds_st<float>(ep.ml_w, m_run);
      lds_st<float>(ep.ml_w + (L_L - L_M), acc[4][0]);
      unsigned old;
      float mj, lj;
      asm volatile("ds_add_rtn_u32 %0, %3, %4 offset:%5\n\tds_read_b32 %1, %3 offset:%6\n\tds_read_b32 %2, %3 offset:%7\n\ts_waitcnt lgkmcnt(0)"
                   : "=&v"(old), "=&v"(mj), "=&v"(lj)
                   : "v"(ep.l4), "v"(last ? 1u : 0u), "i"(L_CNT), "i"(L_M), "i"(L_L)
                   : "memory");
      mine = last && __builtin_amdgcn_readfirstlane(old) == NWAVE - 1;
      const float M = xrow_max(mj);
      const float wt = __builtin_amdgcn_exp2f(mj - M);
      const float L = xrow_sum(wt * lj);
      lds_st<float>(ep.wg_w + (mine ? 0 : 256), L > 0.f ? wt * __builtin_amdgcn_rcpf(L) : 0.f);
      lse = M + __builtin_amdgcn_logf(L);
      asm volatile("" : "+v"(lse));
      __builtin_amdgcn_sched_barrier(0);
    }
    if (g >= N) {
      const int u = g - N;
      transpose4(vf[u]);
#pragma unroll
      for (int k = 0; k < 4; ++k) acc[k] = mfma16(vf[u][k], pf[u], acc[k]);
      __builtin_amdgcn_sched_barrier(0);
    }
  }
}

__global__ void __launch_bounds__(NTHR, 2)
attn_decode(const int* bt_, const int* lens_, unsigned char* ws_, const unsigned short* q_, const unsigned char* kc_,
            const unsigned char* vc_, unsigned short* out_, const void* pad_) {
  __shared__ __attribute__((aligned(16))) char lds_[L_END];
  LAS char* lds = (LAS char*)lds_;
  u32x16 ka;
  asm volatile("s_load_dwordx16 %0, %1, 0x0" : "=s"(ka) : "s"(__builtin_amdgcn_kernarg_segment_ptr()));
  unsigned cw = (threadIdx.x & 63) * 128;
  {
    unsigned long long pc;
    asm volatile("s_getpc_b64 %1\n.Lattn_pc:\n\tv_min_u32 %0, .Lattn_end-.Lattn_pc, %0\n\tglobal_load_dword %0, %0, %1" : "+v"(cw), "=&s"(pc));
  }
  const int bid = blockIdx.x;
  const int sw = (bid >> 3) & 1, team = (bid & 1) * 2 + sw, rank = ((bid >> 1) & 3) | ((bid >> 4) << 2);
  const int w = __builtin_amdgcn_readfirstlane(threadIdx.x >> 6);
  const int b = w & 1, kvh = team ^ b;
  const int slot = (w >> 1) * TEAM + (rank ^ (b * (TEAM - 1)));
  const int bt_off = (b * NBPS + slot) * 4;
  asm volatile("s_waitcnt lgkmcnt(0)" : "+s"(ka) : "s"(bt_off), "s"(WS_EPOCH + team * 256));
#define KPTR(n) (((unsigned long long)ka[2 * (n) + 1] << 32) | ka[2 * (n)])
  int phys[NPW];
  u32x2 lens;
  unsigned epoch;
  static_assert(NPW == 5 && NSLOT * 4 == 0x400, "block-table offsets below");
  asm volatile("s_load_dword %0, %3, %5 offset:0x0\n\ts_load_dwordx2 %1, %4, 0x0\n\ts_load_dword %2, %3, %5 offset:0x400"
               : "=&s"(phys[0]), "=&s"(lens), "=&s"(phys[1])
               : "s"(KPTR(0)), "s"(KPTR(1)), "s"(bt_off));
  auto load_rest = [&]() {
    asm volatile("s_load_dword %0, %4, %6 offset:0x800\n\ts_load_dword %1, %4, %6 offset:0xc00\n\ts_load_dword %2, %4, %6 offset:0x1000\n\t"
                 "s_load_dword %3, %5, %7 offset:0x0\n\ts_waitcnt lgkmcnt(0)"
                 : "=&s"(phys[2]), "=&s"(phys[3]), "=&s"(phys[4]), "=&s"(epoch)
                 : "s"(KPTR(0)), "s"(KPTR(2)), "s"(bt_off), "s"(WS_EPOCH + team * 256));
    epoch += 0x4001u;
  };
  int tid = threadIdx.x;
  asm volatile("" : "+v"(tid));
  const int lane = tid & 63, c = lane >> 4, i = lane & 15;
  lds_st<unsigned>(lds + L_CNT + lane * 4, 0u);
  lds_st<unsigned>(lds + L_LS + 4 + lane * 8, 0u);
  const unsigned char* kc = (const unsigned char*)KPTR(4);
  const unsigned char* vc = (const unsigned char*)KPTR(5);
  u32x4 qf[2];
  {
    __amdgpu_buffer_rsrc_t qr = __builtin_amdgcn_make_buffer_rsrc((void*)KPTR(3), 0, NB * HQ * HD * 2, RSRC3);
#pragma unroll
    for (int s = 0; s < 2; ++s)
      qf[s] = __builtin_amdgcn_raw_buffer_load_b128(qr, (i & 7) * (HD * 2) + c * 32 + (i >> 3) * 16 + s * 128, (b * HQ + kvh * GQ) * HD * 2, 0);
  }
  const int koff = (i >> 1) * (2 * TOK_BYTES) + c * 32 + (i & 1) * 16 + kvh * (HD * 2);
  const int voff = c * 4 * TOK_BYTES + i * 16 + kvh * (HD * 2);
  __builtin_amdgcn_sched_barrier(0);
  asm volatile("s_waitcnt lgkmcnt(0)" : "+s"(phys[0]), "+s"(phys[1]), "+s"(lens) : "v"(koff), "v"(voff));
  float m_run = -1e30f;
  f32x4 acc[5];
#pragma unroll
  for (int k = 0; k < 5; ++k) acc[k] = f32x4{0.f, 0.f, 0.f, 0.f};

  int len = b ? lens[1] : lens[0];
  const bool uni = len <= 0;
  len = uni ? SKV : (len < SKV ? len : SKV);
  const int rem0 = len - slot * PAGE, remk0 = (uni ? 0 : len) - slot * PAGE;

  __amdgpu_buffer_rsrc_t wr = __builtin_amdgcn_make_buffer_rsrc((void*)KPTR(2), 0, WS_END, RSRC3);
  Epi ep;
  auto init_epi = [&]() {
    const int p8 = lane >> 3, e = lane & 7;
    ep.ml_w = lds + L_M + ((w >> 1) * 16 + b * 8 + e) * 4;
    ep.wg_w = lds + L_WG + i * 16 + c * 4;
    ep.acc_w = lds + w * L_SW + lane * 16;
    ep.l4 = lds + lane * 4;
    ep.g_r = lds + L_G + lane * 144 + w * 16;
    const int hd = (w >> 1) & 1, ch = w & 1, cons = (hd * 8 + p8) * 2 + ch;
    const int p0 = (w & 3) * 16 + p8, p1 = p0 + 8;
    const bool mg = w < 4;
    const unsigned lb = (unsigned)(size_t)lds;
    const int pa = mg ? rank : p0, ca = mg ? cons : rank;
    const unsigned o0 = WS_O + ((team * TEAM + pa) * NCONS + ((ca + pa) & (NCONS - 1))) * 128 + e * 16;
    const unsigned o1 = WS_O + ((team * TEAM + p1) * NCONS + ((rank + p1) & (NCONS - 1))) * 128 + e * 16;
    ep.a0 = mg ? lb + L_WG + (hd * 8 + p8) * 16 : WS_F + ((team * 16 + (rank >> 1)) * TEAM + (w & 3) * 16 + i) * 8;
    ep.a1 = (!mg && p0 == rank) ? WS_END : o0;
    ep.a2 = mg ? lb + hd * L_SW + (e >> 1) * L_SK + (ch * 32 + (e & 1) * 8 + p8) * 16 : (p1 == rank ? WS_END : o1);
    ep.a3 = lb + L_G + (mg ? (cons == rank ? rank : TEAM) : (p0 == rank ? TEAM : p0)) * 144 + e * 16;
    ep.a4 = lb + L_G + (p1 == rank ? TEAM : p1) * 144 + e * 16;
    ep.a5 = mg ? 0x4b002000u : (p0 == rank ? 0u : 0x3c000u);
    ep.a6 = mg ? ((rank | lane) == 0 ? WS_EPOCH + team * 256 : WS_END) : (p1 == rank ? 0u : 0x3c000u);
    ep.a7 = lb + L_LS + (mg ? lane : (w & 3) * 16 + i) * 8;
    ep.t2 = (epoch + (e << 15)) & 0x3c000u;
    const int ob = rank >> 4;
    ep.out_off = (e == 0) ? ((ob * HQ + (team ^ ob) * GQ + ((rank >> 1) & 7)) * HD + (rank & 1) * 64 + p8 * 8 + w) * 2 : NB * HQ * HD * 2;
    ep.lse_off = lane < 16 ? WS_F + ((team * 16 + lane) * TEAM + rank) * 8 : WS_END;
    ep.sel_lo = (lane & 8) ? 0x01000c0cu : 0x0c0c0100u;
    ep.sel_hi = (lane & 8) ? 0x03020c0cu : 0x0c0c0302u;
  };

  float lse;
  bool mine;
  do_round<NPW, true>(kc, vc, phys, rem0, remk0, lane, koff, voff, qf, m_run, acc, ep, init_epi, rem0 <= NPW * NSLOT * PAGE, lse, mine,
                      lens, w, bid, wr, load_rest);
  if (__builtin_expect(rem0 > NPW * NSLOT * PAGE, 0)) {
    constexpr int N2 = NBPS / NSLOT - NPW;
    int phys2[N2];
#pragma unroll
    for (int u = 0; u < N2; ++u) phys2[u] = ((const __attribute__((address_space(4))) int*)KPTR(0))[b * NBPS + slot + (NPW + u) * NSLOT];
    do_round<N2, false>(kc, vc, phys2, rem0 - NPW * NSLOT * PAGE, remk0 - NPW * NSLOT * PAGE, lane, koff, voff, qf, m_run, acc, ep,
                        init_epi, true, lse, mine, lens, w, bid, wr, load_rest);
  }
  asm volatile("" ::"v"(cw));
  const unsigned tag = (epoch * 0x9E3779B1u + 0x7F4A7C15u) | 1u;

#pragma unroll
  for (int k = 0; k < 4; ++k) lds_st<f32x4>(ep.acc_w + k * L_SK, acc[k]);

#define DPPF " row_mask:0xf bank_mask:0xf\n\t"
#define WAVE_RED(op)                                             \
  op "_dpp v13, v12, v12 quad_perm:[1,0,3,2]" DPPF "s_nop 1\n\t" \
  op "_dpp v13, v13, v13 quad_perm:[2,3,0,1]" DPPF "s_nop 1\n\t" \
  op "_dpp v13, v13, v13 row_half_mirror" DPPF "s_nop 1\n\t"     \
  op "_dpp v14, v13, v13 row_ror:8" DPPF                         \
  op "_dpp v13, v13, v13 row_ror:8" DPPF "s_nop 1\n\t"           \
  "v_permlane16_swap_b32 v13, v14\n\t"                           \
  op " v15, v13, v14\n\t" op " v13, v13, v14\n\ts_nop 1\n\t"     \
  "v_permlane32_swap_b32 v13, v15\n\t"                           \
  op " v13, v13, v15\n\t"
#define POLL(lo, hi) "v_mov_b32 v" #lo ", 0\n\tv_mov_b32 v" #hi ", 0\n\tbuffer_atomic_or_x2 v[" #lo ":" #hi "], %[a0], %[wr], 0 offen sc0 sc1\n\t"
#define SEEN(lo, hi) "v_cmp_ne_u32 vcc, %[tag], v" #hi "\n\tds_write_b64 %[a7], v[" #lo ":" #hi "]\n\t"
  unsigned spins = 1u << 20, spins2 = 1u << 20;
  __amdgpu_buffer_rsrc_t orr = __builtin_amdgcn_make_buffer_rsrc((void*)KPTR(6), 0, NB * HQ * HD * 2, RSRC3);
  asm volatile(
      "s_cmp_gt_u32 %[w], 3\n\t"
      "s_waitcnt lgkmcnt(0)\n\t"
      "s_barrier\n\t"
      "buffer_atomic_swap_x2 %[lg], %[lo], %[wr], 0 offen sc1\n\t"
      "s_cbranch_scc1 10f\n\t"
      "ds_read_b128 v[4:7], %[a0]\n\t"
      "ds_read_b128 v[8:11], %[a2]\n\t"
      "ds_read_b128 v[12:15], %[a2] offset:256\n\t"
      "ds_read_b128 v[16:19], %[a2] offset:%[o1]\n\t"
      "ds_read_b128 v[20:23], %[a2] offset:%[o1]+256\n\t"
      "ds_read_b128 v[24:27], %[a2] offset:2*%[o1]\n\t"
      "ds_read_b128 v[28:31], %[a2] offset:2*%[o1]+256\n\t"
      "ds_read_b128 v[32:35], %[a2] offset:3*%[o1]\n\t"
      "ds_read_b128 v[36:39], %[a2] offset:3*%[o1]+256\n\t"
      "s_waitcnt lgkmcnt(6)\n\t"
      "v_pk_mul_f32 v[8:9], v[8:9], v[4:5] op_sel_hi:[1,0]\n\t"
      "v_pk_mul_f32 v[10:11], v[10:11], v[4:5] op_sel_hi:[1,0]\n\t"
      "v_pk_mul_f32 v[12:13], v[12:13], v[4:5] op_sel_hi:[1,0]\n\t"
      "v_pk_mul_f32 v[14:15], v[14:15], v[4:5] op_sel_hi:[1,0]\n\t"
      "s_waitcnt lgkmcnt(4)\n\t"
      "v_pk_fma_f32 v[8:9], v[16:17], v[4:5], v[8:9] op_sel:[0,1,0]\n\t"
      "v_pk_fma_f32 v[10:11], v[18:19], v[4:5], v[10:11] op_sel:[0,1,0]\n\t"
      "v_pk_fma_f32 v[12:13], v[20:21], v[4:5], v[12:13] op_sel:[0,1,0]\n\t"
      "v_pk_fma_f32 v[14:15], v[22:23], v[4:5], v[14:15] op_sel:[0,1,0]\n\t"
      "s_waitcnt lgkmcnt(2)\n\t"
      "v_pk_fma_f32 v[8:9], v[24:25], v[6:7], v[8:9] op_sel_hi:[1,0,1]\n\t"
      "v_pk_fma_f32 v[10:11], v[26:27], v[6:7], v[10:11] op_sel_hi:[1,0,1]\n\t"
      "v_pk_fma_f32 v[12:13], v[28:29], v[6:7], v[12:13] op_sel_hi:[1,0,1]\n\t"
      "v_pk_fma_f32 v[14:15], v[30:31], v[6:7], v[14:15] op_sel_hi:[1,0,1]\n\t"
      "s_waitcnt lgkmcnt(0)\n\t"
      "v_pk_fma_f32 v[8:9], v[32:33], v[6:7], v[8:9] op_sel:[0,1,0]\n\t"
      "v_pk_fma_f32 v[10:11], v[34:35], v[6:7], v[10:11] op_sel:[0,1,0]\n\t"
      "v_pk_fma_f32 v[12:13], v[36:37], v[6:7], v[12:13] op_sel:[0,1,0]\n\t"
      "v_pk_fma_f32 v[14:15], v[38:39], v[6:7], v[14:15] op_sel:[0,1,0]\n\t"
      "v_max3_f32 v16, |v8|, |v9|, |v10|\n\t"
      "v_max3_f32 v17, |v11|, |v12|, |v13|\n\t"
      "v_max3_f32 v18, |v14|, |v15|, %[emin]\n\t"
      "v_max3_f32 v16, v16, v17, v18\n\t"
      "v_mov_b32 v20, %[a5]\n\t"
      "v_and_b32 v16, 0x7f800000, v16\n\t"
      "v_sub_u32 v18, 0x84800000, v16\n\t"
      "v_lshrrev_b32 v17, 9, v16\n\t"
      "v_lshrrev_b32 v16, 13, v16\n\t"
      "v_pk_fma_f32 v[8:9], v[8:9], v[18:19], v[20:21] op_sel_hi:[1,0,0]\n\t"
      "v_pk_fma_f32 v[10:11], v[10:11], v[18:19], v[20:21] op_sel_hi:[1,0,0]\n\t"
      "v_pk_fma_f32 v[12:13], v[12:13], v[18:19], v[20:21] op_sel_hi:[1,0,0]\n\t"
      "v_pk_fma_f32 v[14:15], v[14:15], v[18:19], v[20:21] op_sel_hi:[1,0,0]\n\t"
      "v_and_b32 v17, 0x3c000, v17\n\t"
      "v_lshl_or_b32 v4, v9, 18, %[t2]\n\t"
      "v_lshl_or_b32 v5, v11, 18, v17\n\t"
      "v_lshl_or_b32 v6, v13, 18, %[t2]\n\t"
      "v_lshl_or_b32 v7, v15, 18, v16\n\t"
      "v_bfi_b32 v4, %[m14], v8, v4\n\t"
      "v_bfi_b32 v5, %[m14], v10, v5\n\t"
      "v_bfi_b32 v6, %[m14], v12, v6\n\t"
      "v_bfi_b32 v7, %[m14], v14, v7\n\t"
      "buffer_store_dwordx4 v[4:7], %[a1], %[wr], 0 offen sc0 sc1\n\t"
      "s_cmp_gt_u32 %[rk], %[nc]\n\t"
      "s_cbranch_scc1 30f\n\t"
      "ds_write_b128 %[a3], v[4:7]\n\t"
      "s_cmp_lg_u32 %[w], 0\n\t"
      "s_cbranch_scc1 20f\n"
      "1:\n\t"
      "ds_read_b64 v[12:13], %[a7]\n\t"
      "s_sub_u32 %[sp], %[sp], 1\n\t"
      "s_waitcnt lgkmcnt(0)\n\t"
      "s_cbranch_scc1 2f\n\t"
      "v_cmp_ne_u32 vcc, %[tag], v13\n\t"
      "s_sleep 1\n\t"
      "s_cbranch_vccnz 1b\n"
      "2:\n\t"
      "v_mov_b32 v16, %[ep]\n\t"
      "buffer_store_dword v16, %[a6], %[wr], 0 offen sc1\n\t"
      WAVE_RED("v_max_f32")
      "v_sub_f32 v12, v12, v13\n\t"
      "v_exp_f32 v12, v12\n\t"
      "s_nop 1\n\t"
      WAVE_RED("v_add_f32")
      "v_rcp_f32 v13, v13\n\t"
      "v_mul_f32 v12, 0x3a000000, v12\n\t"
      "v_mul_f32 v12, v12, v13\n\t"
      "ds_write_b32 %[l4], v12 offset:%[w2]\n\t"
      "s_branch 20f\n"
      "10:\n\t"
      "s_cmp_gt_u32 %[rk], %[nc]\n\t"
      "s_cbranch_scc1 30f\n\t"
      "s_mov_b64 exec, 0xffff\n\t"
      POLL(12, 13) "s_sleep 4\n\t"
      POLL(14, 15) "s_sleep 4\n\t"
      POLL(16, 17) "s_sleep 4\n"
      "11:\n\t"
      POLL(18, 19) "s_waitcnt vmcnt(3)\n\t" SEEN(12, 13) "s_cbranch_vccz 12f\n\t"
      POLL(12, 13) "s_waitcnt vmcnt(3)\n\t" SEEN(14, 15) "s_cbranch_vccz 12f\n\t"
      POLL(14, 15) "s_waitcnt vmcnt(3)\n\t" SEEN(16, 17) "s_cbranch_vccz 12f\n\t"
      POLL(16, 17) "s_sub_u32 %[sp], %[sp], 1\n\ts_waitcnt vmcnt(3)\n\ts_cbranch_scc1 12f\n\t" SEEN(18, 19) "s_cbranch_vccnz 11b\n"
      "12:\n\t"
      "s_mov_b64 exec, -1\n\t"
      "13:\n\t"
      "buffer_load_dwordx4 v[4:7], %[a1], %[wr], 0 offen sc1\n\t"
      "buffer_load_dwordx4 v[8:11], %[a2], %[wr], 0 offen sc1\n\t"
      "s_sub_u32 %[sq], %[sq], 1\n\t"
      "s_waitcnt vmcnt(1)\n\t"
      "v_bitop3_b32 v20, v4, v6, %[t2] bitop3:0x7e\n\t"
      "ds_write_b128 %[a3], v[4:7]\n\t"
      "v_and_b32 v20, v20, %[a5]\n\t"
      "s_waitcnt vmcnt(0)\n\t"
      "ds_write_b128 %[a4], v[8:11]\n\t"
      "v_bitop3_b32 v21, v8, v10, %[t2] bitop3:0x7e\n\t"
      "v_bitop3_b32 v20, v21, v20, %[a6] bitop3:0xec\n\t"
      "s_cbranch_scc1 20f\n\t"
      "v_cmp_ne_u32 vcc, 0, v20\n\t"
      "s_cbranch_vccnz 13b\n\t"
      "20:\n\t"
      "s_waitcnt lgkmcnt(0)\n\t"
      "s_barrier\n\t"
      "ds_read_b128 v[4:7], %[grd]\n\t"
      "ds_read_b32 v12, %[l4] offset:%[w2]\n\t"
      "v_mov_b32 v16, 0x4b000000\n\t"
      "v_mov_b32 v14, 0xcb002000\n\t"
      "s_waitcnt lgkmcnt(1)\n\t"
      "v_bfe_u32 v13, v5, 14, 4\n\t"
      "v_bfe_u32 v15, v7, 14, 4\n\t"
      "v_and_or_b32 v8, v4, %[m14], v16\n\t"
      "v_and_or_b32 v9, v5, %[m14], v16\n\t"
      "v_lshl_or_b32 v13, v15, 4, v13\n\t"
      "v_and_or_b32 v10, v6, %[m14], v16\n\t"
      "v_and_or_b32 v11, v7, %[m14], v16\n\t"
      "v_lshlrev_b32 v13, 23, v13\n\t"
      "v_alignbit_b32 v4, %[mag], v4, 18\n\t"
      "v_alignbit_b32 v5, %[mag], v5, 18\n\t"
      "v_alignbit_b32 v6, %[mag], v6, 18\n\t"
      "v_alignbit_b32 v7, %[mag], v7, 18\n\t"
      "s_waitcnt lgkmcnt(0)\n\t"
      "v_mul_f32 v12, v12, v13\n\t"
      "v_pk_add_f32 v[8:9], v[8:9], v[14:15] op_sel_hi:[1,0]\n\t"
      "v_pk_add_f32 v[10:11], v[10:11], v[14:15] op_sel_hi:[1,0]\n\t"
      "v_pk_add_f32 v[4:5], v[4:5], v[14:15] op_sel_hi:[1,0]\n\t"
      "v_pk_add_f32 v[6:7], v[6:7], v[14:15] op_sel_hi:[1,0]\n\t"
      "v_pk_mul_f32 v[8:9], v[8:9], v[12:13] op_sel_hi:[1,0]\n\t"
      "v_pk_mul_f32 v[10:11], v[10:11], v[12:13] op_sel_hi:[1,0]\n\t"
      "v_pk_mul_f32 v[4:5], v[4:5], v[12:13] op_sel_hi:[1,0]\n\t"
      "v_pk_mul_f32 v[6:7], v[6:7], v[12:13] op_sel_hi:[1,0]\n\t"
      "v_permlane32_swap_b32 v8, v10\n\t"
      "v_permlane32_swap_b32 v9, v11\n\t"
      "v_permlane32_swap_b32 v4, v6\n\t"
      "v_permlane32_swap_b32 v5, v7\n\t"
      "v_pk_add_f32 v[8:9], v[8:9], v[10:11]\n\t"
      "v_pk_add_f32 v[4:5], v[4:5], v[6:7]\n\t"
      "s_nop 0\n\t"
      "v_permlane16_swap_b32 v8, v9\n\t"
      "v_permlane16_swap_b32 v4, v5\n\t"
      "v_add_f32 v8, v8, v9\n\t"
      "v_add_f32 v4, v4, v5\n\t"
      "s_nop 0\n\t"
      "v_add_f32_dpp v8, v8, v8 row_ror:8 row_mask:0xf bank_mask:0x3\n\t"
      "v_add_f32_dpp v8, v4, v4 row_ror:8 row_mask:0xf bank_mask:0xc\n\t"
      "s_nop 1\n\t"
      "v_add_f32_dpp v8, v8, v8 row_half_mirror" DPPF
      "s_nop 1\n\t"
      "v_add_f32_dpp v8, v8, v8 quad_perm:[2,3,0,1]" DPPF
      "s_nop 1\n\t"
      "v_add_f32_dpp v8, v8, v8 quad_perm:[1,0,3,2]" DPPF
      "v_cvt_pk_bf16_f32 v8, v8, v8\n\t"
      "buffer_store_short v8, %[oo], %[orr], 0 offen\n\t"
      "30:\n"
      ".Lattn_end:"
      : [sp] "+s"(spins), [sq] "+s"(spins2)
      : [wr] "s"(wr), [orr] "s"(orr), [lg] "v"(u32x2{__float_as_uint(lse), tag}), [lo] "v"(mine ? ep.lse_off : WS_END), [w] "s"(w),
        [rk] "s"(rank), [nc] "i"(NCONS - 1), [a0] "v"(ep.a0), [a1] "v"(ep.a1), [a2] "v"(ep.a2), [a3] "v"(ep.a3), [a4] "v"(ep.a4),
        [a5] "v"(ep.a5), [a6] "v"(ep.a6), [a7] "v"(ep.a7), [l4] "v"(ep.l4), [grd] "v"(ep.g_r), [oo] "v"(ep.out_off), [w2] "i"(L_W2),
        [o1] "i"(2 * L_SW), [tag] "s"(tag), [ep] "s"(epoch), [t2] "v"(ep.t2), [m14] "s"(0x3fffu), [mag] "s"(0x4b000000u >> 14),
        [emin] "s"(0x0a000000u)
      : "memory", "vcc", "scc", "v4", "v5", "v6", "v7", "v8", "v9", "v10", "v11", "v12", "v13", "v14", "v15", "v16", "v17", "v18", "v19",
        "v20", "v21", "v22", "v23", "v24", "v25", "v26", "v27", "v28", "v29", "v30", "v31", "v32", "v33", "v34", "v35", "v36", "v37",
        "v38", "v39");
}

extern "C" void kernel_launch(void* const* d_in, const int* in_sizes, int n_in, void* d_out, int out_size, void* d_ws,
                              size_t ws_size, hipStream_t stream) {
  attn_decode<<<NTEAM * TEAM, NTHR, 0, stream>>>((const int*)d_in[3], (const int*)d_in[4], (unsigned char*)d_ws,
                                                 (const unsigned short*)d_in[0], (const unsigned char*)d_in[1],
                                                 (const unsigned char*)d_in[2], (unsigned short*)d_out, d_ws);
}
```
